```python
import torch

def _softmax(x, axis=-1):
    return torch.softmax(x, dim=axis)
def _gelu(x):
    return torch.nn.functional.gelu(x, approximate="tanh")
def _silu(x):
    return torch.nn.functional.silu(x)
def _sigmoid(x):
    return torch.sigmoid(x)
def _layernorm(x, axis=-1, eps=1e-5):
    m = x.mean(dim=axis, keepdim=True)
    v = x.var(dim=axis, keepdim=True, unbiased=False)
    return (x - m) / torch.sqrt(v + eps)
def _scatter_add(tgt, idx, src, axis=0):
    return tgt.clone().index_add_(axis, idx, src)
def _segment_sum(src, seg, n):
    out = torch.zeros((n,) + src.shape[1:], dtype=src.dtype)
    return out.index_add_(0, seg, src)
def _pad(x, pw):
    flat = tuple(v for pair in reversed(pw) for v in pair)
    return torch.nn.functional.pad(x, flat)

kernel_name = 'standalone__gqa_block_cached__near_ridge'

def setup_inputs():

    _w = (lambda a: (lambda t: t.to(torch.bfloat16) if t.is_floating_point() else t)(torch.as_tensor(a)))
    B, Sq, D = 8, 96, 640
    Hq, Hkv, Sk, Dh = 32, 32, 512, 128
    assert Hq % Hkv == 0, (Hq, Hkv)
    return {
        "x": _w((lambda *s: torch.randn(*s, dtype=torch.bfloat16) * 0.8)(B, Sq, D)),
        "k_cache": _w((lambda *s: torch.randn(*s, dtype=torch.bfloat16) * 0.8)(B, Hkv, Sk, Dh)),
        "v_cache": _w((lambda *s: torch.randn(*s, dtype=torch.bfloat16) * 0.05)(B, Hkv, Sk, Dh)),
        "wq": _w((lambda *s: torch.randn(*s, dtype=torch.bfloat16) * 0.8)(D, Hq * Dh) * (D ** -0.5)),
        "wo": _w((lambda *s: torch.randn(*s, dtype=torch.bfloat16) * 0.05)(Hq * Dh, D) * ((Hq * Dh) ** -0.5)),
    }

def reference(x, k_cache, v_cache, wq, wo):
    B, Sq, D = x.shape
    Hq, Dh = 32, 128
    Hkv = k_cache.shape[1]
    rep = Hq // Hkv
    q = (lambda x, *d: x.permute(*d))((x @ wq).reshape(B, Sq, Hq, Dh), 0, 2, 1, 3)
    k = (lambda x, n, axis: torch.repeat_interleave(x, n, dim=axis))(k_cache, rep, axis=1)
    v = (lambda x, n, axis: torch.repeat_interleave(x, n, dim=axis))(v_cache, rep, axis=1)
    scores = torch.einsum("bhqd,bhkd->bhqk", q, k) * (Dh ** -0.5)
    p = _softmax(scores, axis=-1)
    o = torch.einsum("bhqk,bhkd->bhqd", p, v)
    return (lambda x, *d: x.permute(*d))(o, 0, 2, 1, 3).reshape(B, Sq, Hq * Dh) @ wo
```

```cpp
#include <hip/hip_runtime.h>
#include <hip/hip_bf16.h>

typedef __attribute__((ext_vector_type(8))) __bf16 bf16x8;
typedef __attribute__((ext_vector_type(4))) __bf16 bf16x4;
typedef __attribute__((ext_vector_type(2))) __bf16 bf16x2;
typedef __attribute__((ext_vector_type(16))) float f32x16;
typedef __attribute__((ext_vector_type(4))) float f32x4;
typedef __attribute__((ext_vector_type(2))) float f32x2;
typedef __attribute__((ext_vector_type(4))) unsigned u32x4;
typedef __attribute__((ext_vector_type(2))) unsigned u32x2;
#define LAS __attribute__((address_space(3)))
#define DI __device__ __forceinline__
#define MFMA(a, b, c) __builtin_amdgcn_mfma_f32_32x32x16_bf16((a), (b), (c), 0, 0, 0)

#define KV_NT 0
#define W_NT 0

constexpr int NB = 8, SQ = 96, DM = 640, NH = 32, SK = 512, DH = 128;

template <int NT>
DI void glds16(unsigned voff, const void* sbase, unsigned lds_dst) {
  unsigned keep;
  if constexpr (NT)
    asm volatile("s_mov_b32 %0, m0\n\ts_mov_b32 m0, %2\n\ts_nop 0\n\tglobal_load_lds_dwordx4 %1, %3 nt\n\ts_mov_b32 m0, %0"
                 : "=&s"(keep) : "v"(voff), "s"(lds_dst), "s"(sbase) : "memory");
  else
    asm volatile("s_mov_b32 %0, m0\n\ts_mov_b32 m0, %2\n\ts_nop 0\n\tglobal_load_lds_dwordx4 %1, %3\n\ts_mov_b32 m0, %0"
                 : "=&s"(keep) : "v"(voff), "s"(lds_dst), "s"(sbase) : "memory");
}
template <int N> DI void waitvm() { asm volatile("s_waitcnt vmcnt(%0)" ::"i"(N) : "memory"); }
DI void bar() { asm volatile("s_waitcnt lgkmcnt(0)\n\ts_barrier" ::: "memory"); }
DI unsigned pk2(float a, float b) { f32x2 t = {a, b}; bf16x2 p = __builtin_convertvector(t, bf16x2); return __builtin_bit_cast(unsigned, p); }
DI bf16x8 lds128(const char* smem, unsigned off) { return *(const LAS bf16x8*)((const LAS char*)smem + off); }
DI bf16x8 ldstr(const char* smem, unsigned off0, unsigned off1) {
  bf16x4 a = __builtin_amdgcn_ds_read_tr16_b64_v4bf16((LAS bf16x4*)((LAS char*)smem + off0));
  bf16x4 b = __builtin_amdgcn_ds_read_tr16_b64_v4bf16((LAS bf16x4*)((LAS char*)smem + off1));
  return __builtin_shufflevector(a, b, 0, 1, 2, 3, 4, 5, 6, 7);
}
DI float xhalf_max(float v) { auto r = __builtin_amdgcn_permlane32_swap(__float_as_uint(v), __float_as_uint(v), false, false); return fmaxf(__uint_as_float(r[0]), __uint_as_float(r[1])); }
DI float xhalf_sum(float v) { auto r = __builtin_amdgcn_permlane32_swap(__float_as_uint(v), __float_as_uint(v), false, false); return __uint_as_float(r[0]) + __uint_as_float(r[1]); }

constexpr unsigned L_X = 0, L_W = 12288;
constexpr unsigned L_RED = 32768;
constexpr unsigned L_Q = 0;
constexpr unsigned L_P = 0;
constexpr unsigned L_V = 98304;
constexpr unsigned L_MX = 98304 + 57344;
constexpr unsigned L_SM = L_MX + 3072;
constexpr unsigned LDS_BYTES = 163840;

__global__ __launch_bounds__(512, 2) void k_attn(const char* __restrict__ x, const char* __restrict__ kc, const char* __restrict__ vc,
                                                  const char* __restrict__ wq, u32x4* __restrict__ obuf) {
  extern __shared__ __attribute__((aligned(16))) char smem[];
  const int tid = threadIdx.x, lane = tid & 63, w = __builtin_amdgcn_readfirstlane(tid >> 6);
  const int r = lane & 31, hh = lane >> 5;
  const int g = blockIdx.x & 7, idx = blockIdx.x >> 3, b = idx >> 2, i4 = idx & 3, h = 4 * g + i4;
  const unsigned sb = (unsigned)(size_t)smem;
  const char* xb = x + (size_t)b * SQ * DM * 2;
  const char* wqh = wq + (size_t)h * 256;
  const char* kb = kc + ((size_t)(b * NH + h) * SK) * 256;
  const char* vb = vc + ((size_t)(b * NH + h) * SK) * 256;

  bf16x8 kf[2][8];
#pragma unroll
  for (int kt = 0; kt < 2; ++kt)
#pragma unroll
    for (int s = 0; s < 8; ++s) {
      const u32x4* p = (const u32x4*)(kb + (size_t)(64 * w + 32 * kt + r) * 256 + s * 32 + hh * 16);
      u32x4 v = KV_NT ? __builtin_nontemporal_load(p) : *p;
      kf[kt][s] = __builtin_bit_cast(bf16x8, v);
    }

  const int dt = w & 3, ch = w >> 2;
  const int blk = (lane >> 4) & 1, q4 = (lane & 15) >> 2, p4 = lane & 3;
  f32x16 qacc[3] = {};
  for (int st = 0; st < 10; ++st) {
    if (w < 4) {
#pragma unroll
      for (int j = 0; j < 4; ++j) {
        int p = w * 4 + j; int row = 4 * p + (lane >> 4); int chunk = (lane & 15) ^ ((lane >> 4) << 2);
        glds16<W_NT>((unsigned)((64 * st + row) * 8192 + chunk * 16), wqh, sb + L_W + p * 1024);
      }
    } else {
#pragma unroll
      for (int j = 0; j < 3; ++j) {
        int p = (w - 4) * 3 + j; int row = 8 * p + (lane >> 3); int chunk = (lane & 7) ^ ((row >> 1) & 7);
        glds16<W_NT>((unsigned)(row * 1280 + st * 128 + chunk * 16), xb, sb + L_X + p * 1024);
      }
    }
    waitvm<0>();
    bar();
#pragma unroll
    for (int kk = 0; kk < 2; ++kk) {
      int ks = ch * 2 + kk;
      int row0 = 16 * ks + 8 * hh + q4; int cc = 4 * dt + 2 * blk + (p4 >> 1);
      unsigned a0 = L_W + row0 * 256 + ((cc ^ ((row0 & 3) << 2)) * 16) + 8 * (p4 & 1);
      bf16x8 af = ldstr(smem, a0, a0 + 4 * 256);
#pragma unroll
      for (int qb = 0; qb < 3; ++qb) {
        int row = 32 * qb + r;
        bf16x8 bfr = lds128(smem, L_X + row * 128 + (((2 * ks + hh) ^ ((row >> 1) & 7)) * 16));
        qacc[qb] = MFMA(af, bfr, qacc[qb]);
      }
    }
    bar();
  }
  if (ch == 1) {
#pragma unroll
    for (int qb = 0; qb < 3; ++qb)
#pragma unroll
      for (int gq = 0; gq < 4; ++gq) {
        f32x4 v = {qacc[qb][4 * gq], qacc[qb][4 * gq + 1], qacc[qb][4 * gq + 2], qacc[qb][4 * gq + 3]};
        *(LAS f32x4*)((LAS char*)smem + L_RED + ((dt * 12 + qb * 4 + gq) * 64 + lane) * 16) = v;
      }
  }
  bar();
  if (ch == 0) {
#pragma unroll
    for (int qb = 0; qb < 3; ++qb)
#pragma unroll
      for (int gq = 0; gq < 4; ++gq) {
        f32x4 v = *(const LAS f32x4*)((const LAS char*)smem + L_RED + ((dt * 12 + qb * 4 + gq) * 64 + lane) * 16);
        u32x2 o = {pk2(qacc[qb][4 * gq] + v[0], qacc[qb][4 * gq + 1] + v[1]), pk2(qacc[qb][4 * gq + 2] + v[2], qacc[qb][4 * gq + 3] + v[3])};
        int row = 32 * qb + r;
        *(LAS u32x2*)((LAS char*)smem + L_Q + row * 256 + (((4 * dt + gq) ^ (row & 15)) * 16) + 8 * hh) = o;
      }
  }
  bar();
  f32x16 sacc[2][3] = {};
#pragma unroll
  for (int s = 0; s < 8; ++s)
#pragma unroll
    for (int qb = 0; qb < 3; ++qb) {
      int row = 32 * qb + r;
      bf16x8 qf = lds128(smem, L_Q + row * 256 + (((2 * s + hh) ^ (row & 15)) * 16));
      sacc[0][qb] = MFMA(kf[0][s], qf, sacc[0][qb]);
      sacc[1][qb] = MFMA(kf[1][s], qf, sacc[1][qb]);
    }
#pragma unroll
  for (int qb = 0; qb < 3; ++qb) {
    float m = sacc[0][qb][0];
#pragma unroll
    for (int i = 1; i < 16; ++i) m = fmaxf(m, sacc[0][qb][i]);
#pragma unroll
    for (int i = 0; i < 16; ++i) m = fmaxf(m, sacc[1][qb][i]);
    m = xhalf_max(m);
    if (hh == 0) *(LAS float*)((LAS char*)smem + L_MX + (w * 96 + 32 * qb + r) * 4) = m;
  }
  bar();
  const float cs = 0.08838834764831845f * 1.4426950408889634f;
#pragma unroll
  for (int qb = 0; qb < 3; ++qb) {
    float m = -__builtin_inff();
#pragma unroll
    for (int j = 0; j < 8; ++j) m = fmaxf(m, *(const LAS float*)((const LAS char*)smem + L_MX + (j * 96 + 32 * qb + r) * 4));
    float nm = -m * cs;
    float sum = 0.f;
    int row = 32 * qb + r;
#pragma unroll
    for (int kt = 0; kt < 2; ++kt)
#pragma unroll
      for (int gq = 0; gq < 4; ++gq) {
        float e0 = __builtin_amdgcn_exp2f(fmaf(sacc[kt][qb][4 * gq], cs, nm));
        float e1 = __builtin_amdgcn_exp2f(fmaf(sacc[kt][qb][4 * gq + 1], cs, nm));
        float e2 = __builtin_amdgcn_exp2f(fmaf(sacc[kt][qb][4 * gq + 2], cs, nm));
        float e3 = __builtin_amdgcn_exp2f(fmaf(sacc[kt][qb][4 * gq + 3], cs, nm));
        sum += (e0 + e1) + (e2 + e3);
        u32x2 o = {pk2(e0, e1), pk2(e2, e3)};
        int chunk = 8 * w + 4 * kt + gq;
        *(LAS u32x2*)((LAS char*)smem + L_P + row * 1024 + ((chunk ^ (row & 15)) * 16) + 8 * hh) = o;
      }
    sum = xhalf_sum(sum);
    if (hh == 0) *(LAS float*)((LAS char*)smem + L_SM + (w * 96 + 32 * qb + r) * 4) = sum;
  }
  f32x16 oacc[3] = {};
  for (int c = 0; c < 4; ++c) {
#pragma unroll
    for (int j = 0; j < 4; ++j) {
      int row = 4 * w + (lane >> 4); int chunk = (lane & 15) ^ ((lane >> 4) << 2);
      glds16<KV_NT>((unsigned)((128 * c + 32 * j + row) * 256 + chunk * 16), vb, sb + L_V + j * 8192 + w * 1024);
    }
    waitvm<0>();
    bar();
    if (w < 4) {
#pragma unroll
      for (int ks = 0; ks < 8; ++ks) {
        int row0 = 16 * ks + 8 * hh + q4; int cc = 4 * dt + 2 * blk + (p4 >> 1);
        unsigned a0 = L_V + row0 * 256 + ((cc ^ ((row0 & 3) << 2)) * 16) + 8 * (p4 & 1);
        bf16x8 af = ldstr(smem, a0, a0 + 4 * 256);
#pragma unroll
        for (int qb = 0; qb < 3; ++qb) {
          int row = 32 * qb + r; int chunk = 16 * c + 2 * ks + hh;
          bf16x8 pf = lds128(smem, L_P + row * 1024 + ((chunk ^ (row & 15)) * 16));
          oacc[qb] = MFMA(af, pf, oacc[qb]);
        }
      }
    }
    bar();
  }
  if (w < 4) {
#pragma unroll
    for (int qb = 0; qb < 3; ++qb) {
      float l = 0.f;
#pragma unroll
      for (int j = 0; j < 8; ++j) l += *(const LAS float*)((const LAS char*)smem + L_SM + (j * 96 + 32 * qb + r) * 4);
      float inv = 1.0f / l;
#pragma unroll
      for (int s = 0; s < 2; ++s) {
        u32x4 o = {pk2(oacc[qb][8 * s] * inv, oacc[qb][8 * s + 1] * inv), pk2(oacc[qb][8 * s + 2] * inv, oacc[qb][8 * s + 3] * inv),
                   pk2(oacc[qb][8 * s + 4] * inv, oacc[qb][8 * s + 5] * inv), pk2(oacc[qb][8 * s + 6] * inv, oacc[qb][8 * s + 7] * inv)};
        obuf[((size_t)((b * NH + h) * 3 + qb) * 8 + dt * 2 + s) * 64 + lane] = o;
      }
    }
  }
}

__global__ __launch_bounds__(512, 2) void k_oproj(const char* __restrict__ obuf, const char* __restrict__ wo, f32x4* __restrict__ pbuf) {
  extern __shared__ __attribute__((aligned(16))) char smem[];
  const int tid = threadIdx.x, lane = tid & 63, w = __builtin_amdgcn_readfirstlane(tid >> 6);
  const int r = lane & 31, hh = lane >> 5;
  const int g = blockIdx.x & 7, idx = blockIdx.x >> 3, b = idx >> 2, i4 = idx & 3;
  const unsigned sb = (unsigned)(size_t)smem;
  const int blk = (lane >> 4) & 1, q4 = (lane & 15) >> 2, p4 = lane & 3;
  constexpr unsigned L_WO = 0, L_O = 40960;
  f32x16 acc[3] = {};
  const int nt = w >= 4 ? w - 4 : 4;
  for (int hd = 0; hd < 4; ++hd) {
    const char* wob = wo + (size_t)(4 * g + hd) * 128 * 1280 + i4 * 320;
    const char* ob = obuf + (size_t)(b * NH + 4 * g + hd) * 24576;
#pragma unroll
    for (int j = 0; j < 5; ++j) {
      int p = w * 5 + j; int ci = p * 64 + lane; int row = ci / 20, cc = ci % 20;
      glds16<W_NT>((unsigned)(row * 1280 + cc * 16), wob, sb + L_WO + p * 1024);
    }
#pragma unroll
    for (int j = 0; j < 3; ++j) {
      int p = w * 3 + j;
      glds16<0>((unsigned)(p * 1024 + lane * 16), ob, sb + L_O + p * 1024);
    }
    waitvm<0>();
    bar();
    if (w != 3) {
#pragma unroll
      for (int ds = 0; ds < 8; ++ds) {
        int row0 = 16 * ds + 4 * hh + q4; int cc = 4 * nt + 2 * blk + (p4 >> 1);
        unsigned a0 = L_WO + row0 * 320 + cc * 16 + 8 * (p4 & 1);
        bf16x8 af = ldstr(smem, a0, a0 + 8 * 320);
        if (w >= 4) {
#pragma unroll
          for (int qb = 0; qb < 3; ++qb) {
            bf16x8 of = lds128(smem, L_O + (qb * 8 + ds) * 1024 + lane * 16);
            acc[qb] = MFMA(af, of, acc[qb]);
          }
        } else {
          bf16x8 of = lds128(smem, L_O + (w * 8 + ds) * 1024 + lane * 16);
          acc[0] = MFMA(af, of, acc[0]);
        }
      }
    }
    bar();
  }
  f32x4* pb = pbuf + (size_t)(b * 4 + i4) * 8 * 8 * 15 * 32;
  if (w != 3) {
#pragma unroll
    for (int qq = 0; qq < 3; ++qq) {
      if (w >= 4 || qq == 0) {
        int qb = w >= 4 ? qq : w;
        int tile = nt * 3 + qb;
#pragma unroll
        for (int rg = 0; rg < 4; ++rg) {
          f32x4 v = {acc[qq][4 * rg], acc[qq][4 * rg + 1], acc[qq][4 * rg + 2], acc[qq][4 * rg + 3]};
          pb[(((rg * 2 + hh) * 8 + g) * 15 + tile) * 32 + r] = v;
        }
      }
    }
  }
}

__global__ __launch_bounds__(512, 2) void k_red(const f32x4* __restrict__ pbuf, char* __restrict__ out) {
  const int t = threadIdx.x;
  const int g = blockIdx.x & 7, idx = blockIdx.x >> 3, b = idx >> 2, i4 = idx & 3;
  if (t >= 480) return;
  const int tile = t >> 5, r = t & 31, nt = tile / 3, qb = tile % 3, rg = g >> 1, hh = g & 1;
  const f32x4* pb = pbuf + (size_t)(b * 4 + i4) * 8 * 8 * 15 * 32 + (size_t)g * 8 * 15 * 32 + t;
  f32x4 s = pb[0];
#pragma unroll
  for (int j = 1; j < 8; ++j) s += pb[j * 480];
  u32x2 o = {pk2(s[0], s[1]), pk2(s[2], s[3])};
  *(u32x2*)(out + ((size_t)(b * SQ + 32 * qb + r) * DM + 160 * i4 + 32 * nt + 8 * rg + 4 * hh) * 2) = o;
}

extern "C" void kernel_launch(void* const* d_in, const int* in_sizes, int n_in, void* d_out, int out_size, void* d_ws, size_t ws_size,
                              hipStream_t stream) {
  const char* x = (const char*)d_in[0];
  const char* kc = (const char*)d_in[1];
  const char* vc = (const char*)d_in[2];
  const char* wq = (const char*)d_in[3];
  const char* wo = (const char*)d_in[4];
  char* ws = (char*)d_ws;
  u32x4* obuf = (u32x4*)ws;
  f32x4* pbuf = (f32x4*)(ws + (8u << 20));
  (void)hipFuncSetAttribute((const void*)k_attn, hipFuncAttributeMaxDynamicSharedMemorySize, LDS_BYTES);
  (void)hipFuncSetAttribute((const void*)k_oproj, hipFuncAttributeMaxDynamicSharedMemorySize, 65536);
  hipLaunchKernelGGL(k_attn, dim3(256), dim3(512), LDS_BYTES, stream, x, kc, vc, wq, obuf);
  hipLaunchKernelGGL(k_oproj, dim3(256), dim3(512), 65536, stream, (const char*)obuf, wo, pbuf);
  hipLaunchKernelGGL(k_red, dim3(256), dim3(512), 0, stream, (const f32x4*)pbuf, (char*)d_out);
}
```

```cpp
#include <hip/hip_runtime.h>
#include <hip/hip_bf16.h>

typedef __attribute__((ext_vector_type(8))) __bf16 bf16x8;
typedef __attribute__((ext_vector_type(4))) __bf16 bf16x4;
typedef __attribute__((ext_vector_type(2))) __bf16 bf16x2;
typedef __attribute__((ext_vector_type(16))) float f32x16;
typedef __attribute__((ext_vector_type(4))) float f32x4;
typedef __attribute__((ext_vector_type(2))) float f32x2;
typedef __attribute__((ext_vector_type(4))) unsigned u32x4;
typedef __attribute__((ext_vector_type(2))) unsigned u32x2;
#define LAS __attribute__((address_space(3)))
#define GAS __attribute__((address_space(1)))
#define DI __device__ __forceinline__
#define AI __attribute__((always_inline))
#define MFMA(a, b, c) __builtin_amdgcn_mfma_f32_32x32x16_bf16((a), (b), (c), 0, 0, 0)
#define SB() __builtin_amdgcn_sched_barrier(0)

#define KV_NT 1
#define STAMP(i) do {} while (0)

constexpr int SQ = 96, DM = 640, NH = 32, SK = 512;
constexpr unsigned KB = 1024;
constexpr unsigned LDS_BYTES = 163840;

constexpr size_t WS_EPOCH = 0;
constexpr size_t WS_F1 = 4096;
constexpr size_t WS_F2 = 16384;
constexpr size_t WS_O = 1u << 20;
constexpr size_t WS_P = 8u << 20;

#define DMA_PRE "s_mov_b32 m0, %0\n\ts_nop 0\n\t"
template <int NT> DI void dma4(unsigned v0, unsigned v1, unsigned v2, unsigned v3, const void* sbase, unsigned lds_dst) {
  lds_dst = __builtin_amdgcn_readfirstlane(lds_dst);
  if constexpr (NT)
    asm volatile(DMA_PRE "global_load_lds_dwordx4 %2, %1 nt\n\tglobal_load_lds_dwordx4 %3, %1 offset:1024 nt\n\tglobal_load_lds_dwordx4 %4, %1 offset:2048 nt\n\tglobal_load_lds_dwordx4 %5, %1 offset:3072 nt"
                 :: "s"(lds_dst), "s"(sbase), "v"(v0), "v"(v1), "v"(v2), "v"(v3) : "memory");
  else
    asm volatile(DMA_PRE "global_load_lds_dwordx4 %2, %1\n\tglobal_load_lds_dwordx4 %3, %1 offset:1024\n\tglobal_load_lds_dwordx4 %4, %1 offset:2048\n\tglobal_load_lds_dwordx4 %5, %1 offset:3072"
                 :: "s"(lds_dst), "s"(sbase), "v"(v0), "v"(v1), "v"(v2), "v"(v3) : "memory");
}
DI void dma3(unsigned v0, unsigned v1, unsigned v2, const void* sbase, unsigned lds_dst) {
  lds_dst = __builtin_amdgcn_readfirstlane(lds_dst);
  asm volatile(DMA_PRE "global_load_lds_dwordx4 %2, %1\n\tglobal_load_lds_dwordx4 %3, %1 offset:1024\n\tglobal_load_lds_dwordx4 %4, %1 offset:2048"
               :: "s"(lds_dst), "s"(sbase), "v"(v0), "v"(v1), "v"(v2) : "memory");
}
template <int NT> DI void dma2(unsigned v0, unsigned v1, const void* sbase, unsigned lds_dst) {
  lds_dst = __builtin_amdgcn_readfirstlane(lds_dst);
  if constexpr (NT)
    asm volatile(DMA_PRE "global_load_lds_dwordx4 %2, %1 nt\n\tglobal_load_lds_dwordx4 %3, %1 offset:1024 nt"
                 :: "s"(lds_dst), "s"(sbase), "v"(v0), "v"(v1) : "memory");
  else
    asm volatile(DMA_PRE "global_load_lds_dwordx4 %2, %1\n\tglobal_load_lds_dwordx4 %3, %1 offset:1024"
                 :: "s"(lds_dst), "s"(sbase), "v"(v0), "v"(v1) : "memory");
}
template <int N> DI void waitvm() { asm volatile("s_waitcnt vmcnt(%0)" ::"i"(N) : "memory"); }
DI void bar() { asm volatile("s_waitcnt lgkmcnt(0)\n\ts_barrier" ::: "memory"); }
DI void waitlds() { asm volatile("s_waitcnt lgkmcnt(0)" ::: "memory"); }
DI unsigned pk2(float a, float b) { f32x2 t = {a, b}; bf16x2 p = __builtin_convertvector(t, bf16x2); return __builtin_bit_cast(unsigned, p); }
DI bf16x8 lds128(const char* smem, unsigned off) { return *(const LAS bf16x8*)((const LAS char*)smem + off); }
DI void sts128(char* smem, unsigned off, u32x4 v) { *(LAS u32x4*)((LAS char*)smem + off) = v; }
DI bf16x8 ldstr(const char* smem, unsigned off0, unsigned off1) {
  bf16x4 a = __builtin_amdgcn_ds_read_tr16_b64_v4bf16((LAS bf16x4*)((LAS char*)smem + off0));
  bf16x4 b = __builtin_amdgcn_ds_read_tr16_b64_v4bf16((LAS bf16x4*)((LAS char*)smem + off1));
  return __builtin_shufflevector(a, b, 0, 1, 2, 3, 4, 5, 6, 7);
}
DI float xhalf_max(float v) { auto r = __builtin_amdgcn_permlane32_swap(__float_as_uint(v), __float_as_uint(v), false, false); return fmaxf(__uint_as_float(r[0]), __uint_as_float(r[1])); }
DI float xhalf_sum(float v) { auto r = __builtin_amdgcn_permlane32_swap(__float_as_uint(v), __float_as_uint(v), false, false); return __uint_as_float(r[0]) + __uint_as_float(r[1]); }
DI u32x4 pack8(const f32x16& a, int s) {
  u32x4 o = {pk2(a[8 * s], a[8 * s + 1]), pk2(a[8 * s + 2], a[8 * s + 3]), pk2(a[8 * s + 4], a[8 * s + 5]), pk2(a[8 * s + 6], a[8 * s + 7])};
  return o;
}
template <int NM, int ND> DI void ilv() {
#pragma unroll
  for (int i = 0; i < NM; ++i) { __builtin_amdgcn_sched_group_barrier(0x008, 1, 0); __builtin_amdgcn_sched_group_barrier(0x100, ND, 0); }
  __builtin_amdgcn_sched_barrier(0);
}
DI void st_sc1(__amdgpu_buffer_rsrc_t rs, unsigned off, u32x4 v) { __builtin_amdgcn_raw_buffer_store_b128(v, rs, off, 0, 16); }
DI u32x4 ld_sc1(__amdgpu_buffer_rsrc_t rs, unsigned off) { return __builtin_amdgcn_raw_buffer_load_b128(rs, off, 0, 16); }
#define RLX_AGENT __ATOMIC_RELAXED, __HIP_MEMORY_SCOPE_AGENT
constexpr unsigned SPIN_CAP = 1u << 22;

constexpr unsigned S(int i) { return 8 * KB * i; }
constexpr unsigned area(int a) { return a == 0 ? S(12) : a == 1 ? S(16) : a == 2 ? S(8) : a == 3 ? S(0) : S(4); }
constexpr unsigned qring(int s0) { return area(s0 % 5); }
constexpr unsigned QIMG = S(8), PB = S(8), PA = S(17), MISC = S(11);
constexpr unsigned L_MX = MISC, L_SM = MISC + 3072, L_TAG = MISC + 6144;
constexpr unsigned vslot(int j) { return j < 5 ? S(12 + j) : j < 13 ? S(j - 5) : S(12 + j - 13); }
template <int TYPE> constexpr unsigned karea(int q) { return TYPE == 0 ? (q < 2 ? area(q) : area(q + 1)) : area(q + 1); }
constexpr unsigned oblk(int jj, int qb) {
  return jj == 0 ? (qb == 0 ? S(7) : qb == 1 ? S(12) : S(13)) : jj == 1 ? (qb == 0 ? S(14) : qb == 1 ? S(8) : S(9))
       : jj == 2 ? (qb == 0 ? S(10) : qb == 1 ? S(15) : S(16)) : (qb == 0 ? S(0) : qb == 1 ? S(1) : S(6));
}
constexpr unsigned WOB = S(2);

struct Ptrs {
  const char* xb; const char* wqh; const char* kb; const char* vb; const char* wo; char* ws; char* out;
};

template <int TYPE, int ROLE>
DI void body(char* smem, const Ptrs& P, const int b, const int g, const int i4, const int rot, const int w) {
  const int tid = threadIdx.x, lane = tid & 63;
  const int r = lane & 31, hh = lane >> 5;
  const unsigned sb = (unsigned)(size_t)smem;
  const int blk = (lane >> 4) & 1, q4 = (lane & 15) >> 2, p4 = lane & 3;
  const int dt = w & 3;
  STAMP(0);
  unsigned epoch = 0;
  if constexpr (ROLE == 0) epoch = __hip_atomic_load((GAS unsigned*)(P.ws + WS_EPOCH + b * 256), RLX_AGENT);

  const unsigned u = lane >> 4;
  const unsigned vo_tr = u * 256 + (((lane & 15) ^ (u << 2)) * 16);
  const unsigned vo_wq = u * 8192 + (((lane & 15) ^ (u << 2)) * 16);
  const unsigned vo_k = u * 256 + (((lane & 15) ^ u) * 16);
  const unsigned vo_x = (lane >> 3) * 1280 + (((lane & 7) ^ u) * 16);
  const unsigned wq0 = vo_wq + dt * 16 * 8192, wq1 = wq0 + (32768 - 1024), wq2 = wq0 + 2 * (32768 - 1024), wq3 = wq0 + 3 * (32768 - 1024);
  const unsigned xb0 = (vo_x ^ (((3 * dt) & 1) << 6)) + dt * 3 * 10240, xb1 = (vo_x ^ (((3 * dt + 1) & 1) << 6)) + dt * 3 * 10240 + (10240 - 1024),
                 xb2 = xb0 + 2 * (10240 - 1024);
  const unsigned k0v = vo_k + dt * 8192, k1v = k0v ^ 64, k2v = k0v ^ 128, k3v = k0v ^ 192;
  const unsigned vqv = vo_tr + dt * 8192, vtv = vo_tr + dt * 2048;
  auto issue_q = [&](int s0) AI {
    if constexpr (ROLE == 1) {
      int s = s0 + rot; if (s >= 10) s -= 10;
      dma4<0>(wq0, wq1, wq2, wq3, P.wqh + (size_t)s * (64 * 8192), sb + qring(s0) + 4 * dt * KB);
      dma3(xb0, xb1, xb2, P.xb + s * 128, sb + qring(s0) + 16 * KB + 3 * dt * KB);
    }
  };
  auto issue_kq = [&](int q) AI {
    if constexpr (ROLE == 1) {
      dma4<KV_NT>(k0v, k1v, k2v, k3v, P.kb + q * 32768, sb + karea<TYPE>(q) + dt * 8 * KB);
      dma4<KV_NT>(k0v, k1v, k2v, k3v, P.kb + q * 32768 + 4096, sb + karea<TYPE>(q) + dt * 8 * KB + 4096);
    }
  };
  auto issue_v = [&](int j) AI {
    if constexpr (ROLE == 1) dma2<KV_NT>(vtv, vtv, P.vb + j * 8192, sb + vslot(j) + dt * 2 * KB);
  };
  auto issue_vh = [&](int j0, int half) AI {
    if constexpr (ROLE == 1) dma4<KV_NT>(vqv, vqv, vqv, vqv, P.vb + j0 * 8192 + half * 4096, sb + vslot(j0) + dt * 8 * KB + half * 4096);
  };
#define LWAIT(n) do { if constexpr (ROLE == 1) waitvm<n>(); } while (0)
  bf16x8 kf[2][8];
  auto pull_k = [&](int q) AI {
    if ((q >> 1) != ROLE) return;
    const unsigned slot = karea<TYPE>(q) + dt * 8 * KB + r * 256;
#pragma unroll
    for (int s = 0; s < 8; ++s) kf[q & 1][s] = lds128(smem, slot + (((2 * s + hh) ^ (r & 15)) * 16));
  };

  if constexpr (TYPE == 0) {
    issue_q(0); issue_q(1); issue_q(2);
  } else {
    issue_kq(0); issue_kq(1); issue_kq(2);
    LWAIT(16); bar(); issue_kq(3); pull_k(0);
    LWAIT(16); bar(); issue_q(0); pull_k(1);
    LWAIT(15); pull_k(2); issue_q(1);
    LWAIT(14); pull_k(3);
    STAMP(1);
  }
  {
    f32x16 qacc[3] = {};
    const int p4s = ((p4 & 1) << 1) | (p4 >> 1);
    const unsigned a_off = (8 * hh + q4) * 256 + (((4 * dt + 2 * blk + (p4s >> 1)) ^ (q4 << 2)) * 16) + 8 * (p4s & 1);
    const unsigned xrow0 = r * 128 + 16 * KB, xsw = ((r >> 1) & 7);
    bf16x8 fa[4], fb[4][3];
    auto read_q = [&](int s0, int ks) AI {
      if constexpr (ROLE == 0) {
        const unsigned base = qring(s0);
        fa[ks] = ldstr(smem, base + a_off + ks * 4096, base + a_off + ks * 4096 + 1024);
#pragma unroll
        for (int j = 0; j < 3; ++j) fb[ks][j] = lds128(smem, base + xrow0 + j * 4096 + (((2 * ks + hh) ^ xsw) * 16));
      }
    };
    auto mma_q = [&](int ks) AI {
      if constexpr (ROLE == 0) {
#pragma unroll
        for (int j = 0; j < 3; ++j) qacc[j] = MFMA(fa[ks], fb[ks][j], qacc[j]);
      }
    };
#pragma unroll
    for (int s0 = 0; s0 < 10; ++s0) {
      if constexpr (TYPE == 0) {
        if (s0 < 8) LWAIT(14); else if (s0 == 8) LWAIT(15); else LWAIT(16);
        bar();
        if (s0 < 7) issue_q(s0 + 3); else issue_kq(s0 - 7);
      } else {
        if (s0 == 0) LWAIT(7); else if (s0 < 8) LWAIT(14); else if (s0 == 8) LWAIT(7); else LWAIT(8);
        bar();
        if (s0 == 0) issue_q(2);
        if (s0 < 7) issue_q(s0 + 3); else if (s0 == 8) { issue_vh(0, 0); issue_vh(0, 1); } else if (s0 == 9) issue_v(4);
      }
      SB();
      read_q(s0, 0); read_q(s0, 1);
      if (s0 > 0) mma_q(2);
      ilv<3, 4>();
      read_q(s0, 2);
      if (s0 > 0) mma_q(3);
      ilv<3, 2>();
      read_q(s0, 3);
      mma_q(0);
      ilv<3, 2>();
      mma_q(1);
      SB();
    }
    mma_q(2); mma_q(3);
    if constexpr (ROLE == 0) {
#pragma unroll
      for (int j = 0; j < 3; ++j)
#pragma unroll
        for (int sx = 0; sx < 2; ++sx) sts128(smem, QIMG + (j * 8 + 2 * dt + sx) * KB + lane * 16, pack8(qacc[j], sx));
    }
  }
  const unsigned tag0 = epoch * 1664525u + 1013904223u;
  if constexpr (ROLE == 0) { if (tid == 0) *(LAS unsigned*)((LAS char*)smem + L_TAG) = tag0; }
  f32x16 sacc[2][3] = {};
  bf16x8 qf[2][6];
  auto rd_q6 = [&](int st) AI {
#pragma unroll
    for (int i = 0; i < 6; ++i) qf[st & 1][i] = lds128(smem, QIMG + ((i % 3) * 8 + 2 * st + i / 3) * KB + lane * 16);
  };
  auto s_tile = [&](int kt) AI {
    rd_q6(0); SB();
#pragma unroll
    for (int st = 0; st < 4; ++st) {
      if (st < 3) rd_q6(st + 1);
#pragma unroll
      for (int i = 0; i < 6; ++i) sacc[kt][i % 3] = MFMA(kf[kt][2 * st + i / 3], qf[st & 1][i], sacc[kt][i % 3]);
      ilv<6, 1>();
    }
  };
  auto s_tile2 = [&]() AI {
    rd_q6(0); SB();
#pragma unroll
    for (int st = 0; st < 4; ++st) {
      if (st < 3) rd_q6(st + 1);
#pragma unroll
      for (int i = 0; i < 6; ++i) {
        sacc[0][i % 3] = MFMA(kf[0][2 * st + i / 3], qf[st & 1][i], sacc[0][i % 3]);
        sacc[1][i % 3] = MFMA(kf[1][2 * st + i / 3], qf[st & 1][i], sacc[1][i % 3]);
      }
      ilv<6, 1>();
    }
  };
  auto put_max = [&]() AI {
#pragma unroll
    for (int qb = 0; qb < 3; ++qb) {
      float m = sacc[0][qb][0];
#pragma unroll
      for (int kt = 0; kt < 2; ++kt)
#pragma unroll
        for (int i = 0; i < 16; ++i) m = fmaxf(m, sacc[kt][qb][i]);
      m = xhalf_max(m);
      if (hh == 0) *(LAS float*)((LAS char*)smem + L_MX + ((32 * qb + r) * 8 + w) * 4) = m;
    }
  };
  const float cs = 0.08838834764831845f * 1.4426950408889634f;
  float nm[3], lsum[3] = {0.f, 0.f, 0.f};
  auto max4 = [&](int qb, int half) AI {
    f32x4 m = *(const LAS f32x4*)((const LAS char*)smem + L_MX + (32 * qb + r) * 32 + half * 16);
    return fmaxf(fmaxf(m[0], m[1]), fmaxf(m[2], m[3]));
  };
  u32x4 pr[2][3][2];
  auto exp_tile = [&](int kt) AI {
#pragma unroll
    for (int qb = 0; qb < 3; ++qb) {
#pragma unroll
      for (int i = 0; i < 16; ++i) { float e = __builtin_amdgcn_exp2f(fmaf(sacc[kt][qb][i], cs, nm[qb])); sacc[kt][qb][i] = e; lsum[qb] += e; }
#pragma unroll
      for (int sx = 0; sx < 2; ++sx) pr[kt][qb][sx] = pack8(sacc[kt][qb], sx);
    }
  };
  auto put_p = [&](int kt) AI {
#pragma unroll
    for (int qb = 0; qb < 3; ++qb)
#pragma unroll
      for (int sx = 0; sx < 2; ++sx) sts128(smem, (kt == 0 ? PA : PB) + dt * 6 * KB + lane * 16 + (sx * 3 + qb) * KB, pr[kt][qb][sx]);
  };
  auto put_sum = [&]() AI {
#pragma unroll
    for (int qb = 0; qb < 3; ++qb) {
      float s = xhalf_sum(lsum[qb]);
      if (hh == 0) *(LAS float*)((LAS char*)smem + L_SM + ((32 * qb + r) * 8 + w) * 4) = s;
    }
  };

  if constexpr (TYPE == 0) {
    LWAIT(16); bar();
    STAMP(2);
    issue_kq(3);
    if constexpr (ROLE == 0) { pull_k(0); s_tile(0); }
    LWAIT(16); bar();
    if constexpr (ROLE == 0) { pull_k(1); s_tile(1); put_max(); }
    bar();
    STAMP(1);
    if constexpr (ROLE == 1) {
      waitvm<8>(); pull_k(2); issue_vh(0, 0); s_tile(0); issue_vh(0, 1);
      waitvm<8>(); pull_k(3); waitlds(); issue_v(4); s_tile(1); put_max();
      issue_vh(5, 0); issue_vh(5, 1);
      waitvm<10>();
    }
  } else {
    bar();
    STAMP(2);
    if constexpr (ROLE == 0) { s_tile2(); put_max(); } else { issue_vh(5, 0); }
    bar();
    if constexpr (ROLE == 1) {
      issue_vh(5, 1); s_tile2(); put_max();
      waitvm<10>();
    }
  }
  float m0[3];
  if constexpr (ROLE == 0) {
#pragma unroll
    for (int qb = 0; qb < 3; ++qb) { m0[qb] = max4(qb, 0); nm[qb] = -m0[qb] * cs; }
    exp_tile(0); put_p(0); exp_tile(1); put_sum();
  }
  STAMP(3);
  bar();
  STAMP(4);
  f32x16 oa[2][3] = {};
  const unsigned v_off = (4 * hh + q4) * 256 + (((4 * dt + 2 * blk + (p4 >> 1)) ^ (q4 << 2)) * 16) + 8 * (p4 & 1);
  bf16x8 vf[2][2], pf[2][2][3];
  auto rd_v = [&](int j) AI {
    const unsigned vbase = vslot(j) + v_off;
#pragma unroll
    for (int sx = 0; sx < 2; ++sx) vf[j & 1][sx] = ldstr(smem, vbase + sx * 4096, vbase + sx * 4096 + 2048);
  };
  auto rd_p = [&](int j) AI {
    const unsigned pbase = (((j >> 2) & 1) ? PB : PA) + (j & 3) * 6 * KB + lane * 16;
#pragma unroll
    for (int sx = 0; sx < 2; ++sx)
#pragma unroll
      for (int qb = 0; qb < 3; ++qb) pf[j & 1][sx][qb] = lds128(smem, pbase + (sx * 3 + qb) * KB);
  };
  auto mma_pv = [&](int j) AI {
#pragma unroll
    for (int sx = 0; sx < 2; ++sx)
#pragma unroll
      for (int qb = 0; qb < 3; ++qb) oa[j >> 3][qb] = MFMA(vf[j & 1][sx], pf[j & 1][sx][qb], oa[j >> 3][qb]);
  };
  const unsigned wo0 = (lane >> 2) * 1280 + (lane & 3) * 16 + dt * 64, wo1 = wo0 + (20480 - 1024), wo2 = wo0 + 2 * (20480 - 1024), wo3 = wo0 + 3 * (20480 - 1024);
  const unsigned wst0 = dt == 0 ? S(15) : dt == 1 ? S(16) : dt == 2 ? S(0) : S(1);
  const unsigned wst1 = dt == 0 ? S(6) : S(16 + dt);
  const char* wob = P.wo + (size_t)(4 * g) * 128 * 1280 + i4 * 320;
  auto issue_wa = [&](int jj, int half) AI {
    const int hd = (i4 + jj) & 3;
    dma4<0>(wo0, wo1, wo2, wo3, wob + (size_t)(hd * 128 + half * 64) * 1280, sb + ((jj & 1) ? wst1 : wst0) + half * 4096);
  };
  auto issue_wb = [&]() AI {
    const int hd = (i4 + dt) & 3;
    const unsigned d = 256 - dt * 64;
    dma4<0>(wo0 + d, wo1 + d, wo2 + d, wo3 + d, wob + (size_t)(hd * 128) * 1280, sb + WOB + dt * 8 * KB);
    dma4<0>(wo0 + d, wo1 + d, wo2 + d, wo3 + d, wob + (size_t)(hd * 128 + 64) * 1280, sb + WOB + dt * 8 * KB + 4096);
  };
  const unsigned w_off = (4 * hh + q4) * 64 + (2 * blk + (p4 >> 1)) * 16 + 8 * (p4 & 1);
  bf16x8 wf[4][8];
  auto pull_wa = [&](int jj) AI {
    const unsigned base = ((jj & 1) ? wst1 : wst0) + w_off;
#pragma unroll
    for (int ds = 0; ds < 8; ++ds) wf[jj][ds] = ldstr(smem, base + ds * KB, base + ds * KB + 512);
  };

  float sc0[3], sc1[3];
  if constexpr (ROLE == 0) {
    rd_v(0); rd_p(0); put_p(1); SB();
    rd_v(1); rd_p(1); mma_pv(0); ilv<6, 2>();
    rd_v(2); rd_p(2); mma_pv(1); ilv<6, 2>();
    rd_v(3); rd_p(3); mma_pv(2); ilv<6, 2>();
    bar(); SB();
    rd_v(4); rd_p(4); mma_pv(3); ilv<6, 2>();
    rd_v(5); rd_p(5); mma_pv(4); ilv<6, 2>();
    rd_v(6); rd_p(6); mma_pv(5); ilv<6, 2>();
    rd_v(7); rd_p(7); mma_pv(6); ilv<6, 2>();
    bar(); SB();
    STAMP(5);
    rd_v(8); rd_p(8); mma_pv(7); ilv<6, 2>();
    rd_v(9); rd_p(9); mma_pv(8); ilv<6, 2>();
    rd_v(10); rd_p(10); mma_pv(9); ilv<6, 2>();
    rd_v(11); rd_p(11); mma_pv(10); ilv<6, 2>();
#pragma unroll
    for (int qb = 0; qb < 3; ++qb) {
      const float m = fmaxf(m0[qb], max4(qb, 1));
      const float al = __builtin_amdgcn_exp2f((m0[qb] - m) * cs);
      f32x4 l0 = *(const LAS f32x4*)((const LAS char*)smem + L_SM + (32 * qb + r) * 32);
      f32x4 l1 = *(const LAS f32x4*)((const LAS char*)smem + L_SM + (32 * qb + r) * 32 + 16);
      const float l = al * ((l0[0] + l0[1]) + (l0[2] + l0[3])) + ((l1[0] + l1[1]) + (l1[2] + l1[3]));
      sc1[qb] = 1.0f / l; sc0[qb] = al * sc1[qb];
    }
    bar(); SB();
    rd_v(12); rd_p(12); mma_pv(11); ilv<6, 2>();
    rd_v(13); rd_p(13); mma_pv(12); ilv<6, 2>();
#pragma unroll
    for (int qb = 0; qb < 3; ++qb)
#pragma unroll
      for (int i = 0; i < 16; ++i) oa[0][qb][i] *= sc0[qb];
    bar(); SB();
    rd_v(14); rd_p(14); mma_pv(13); ilv<6, 2>();
    rd_p(15); mma_pv(14); ilv<6, 1>();
    bar(); SB();
    STAMP(6);
    rd_v(15); mma_pv(15);
  } else {
#pragma unroll
    for (int qb = 0; qb < 3; ++qb) nm[qb] = -fmaxf(max4(qb, 0), max4(qb, 1)) * cs;
    issue_vh(9, 0);
    exp_tile(0);
    issue_vh(9, 1);
    waitvm<8>();
    bar();
    put_p(0);
    issue_v(13);
    exp_tile(1); put_sum();
    issue_v(14); issue_v(15);
    waitvm<6>();
    bar();
    put_p(1);
    issue_wa(0, 0);
    waitvm<8>();
    bar();
    issue_wa(0, 1);
    waitvm<10>();
    bar();
    waitvm<8>();
    bar();
    issue_wb(); issue_wa(1, 0); issue_wa(1, 1);
  }
  const unsigned tag = __builtin_amdgcn_readfirstlane(*(const LAS unsigned*)((const LAS char*)smem + L_TAG));
  const __amdgpu_buffer_rsrc_t prs = __builtin_amdgcn_make_buffer_rsrc(P.ws + WS_P + (size_t)(b * 4 + i4) * (8 * 8 * 15 * 512), 0, 8 * 8 * 15 * 512, 0x00020000);
  f32x16 acc[3] = {};
  if constexpr (ROLE == 0) {
    const __amdgpu_buffer_rsrc_t ors = __builtin_amdgcn_make_buffer_rsrc(P.ws + WS_O + (size_t)(b * NH + 4 * g) * 24576, 0, 4 * 24576, 0x00020000);
#pragma unroll
    for (int qb = 0; qb < 3; ++qb) {
#pragma unroll
      for (int i = 0; i < 16; ++i) oa[1][qb][i] = fmaf(oa[1][qb][i], sc1[qb], oa[0][qb][i]);
#pragma unroll
      for (int sx = 0; sx < 2; ++sx) {
        const u32x4 o = pack8(oa[1][qb], sx);
        st_sc1(ors, i4 * 24576 + (qb * 8 + dt * 2 + sx) * KB + lane * 16, o);
        sts128(smem, oblk(0, qb) + (dt * 2 + sx) * KB + lane * 16, o);
      }
    }
    STAMP(7);
    bar();
    waitvm<0>();
    GAS unsigned* f1 = (GAS unsigned*)(P.ws + WS_F1 + (size_t)(b * 8 + g) * 64 + dt * 16);
    if (lane == 0) __hip_atomic_store(f1 + i4, tag, RLX_AGENT);
    STAMP(8);
#pragma unroll
    for (int jj = 1; jj < 4; ++jj) {
      const int hd = (i4 + jj) & 3;
      for (unsigned spin = 0; spin < SPIN_CAP; ++spin) {
        if ((unsigned)__builtin_amdgcn_readfirstlane(__hip_atomic_load(f1 + hd, RLX_AGENT)) == tag) break;
        __builtin_amdgcn_s_sleep(1);
      }
      __builtin_amdgcn_fence(__ATOMIC_ACQUIRE, "wavefront");
      u32x4 t[6];
#pragma unroll
      for (int i = 0; i < 6; ++i) t[i] = ld_sc1(ors, hd * 24576 + ((i >> 1) * 8 + dt * 2 + (i & 1)) * KB + lane * 16);
      if (jj >= 2) bar();
#pragma unroll
      for (int i = 0; i < 6; ++i) sts128(smem, oblk(jj, i >> 1) + (dt * 2 + (i & 1)) * KB + lane * 16, t[i]);
      bar();
      STAMP(8 + jj);
    }
    if (w < 3) {
#pragma unroll
      for (int jj = 0; jj < 4; ++jj)
#pragma unroll
        for (int ds = 0; ds < 8; ++ds) {
          const unsigned wb = WOB + (jj * 8 + ds) * KB + w_off;
          bf16x8 af = ldstr(smem, wb, wb + 512);
          const unsigned ob = (w == 0 ? oblk(jj, 0) : w == 1 ? oblk(jj, 1) : oblk(jj, 2)) + ds * KB + lane * 16;
          acc[0] = MFMA(af, lds128(smem, ob), acc[0]);
        }
    }
  } else {
    auto mma_o = [&](int jj) AI {
#pragma unroll
      for (int ds = 0; ds < 8; ++ds)
#pragma unroll
        for (int qb = 0; qb < 3; ++qb) acc[qb] = MFMA(wf[jj][ds], lds128(smem, oblk(jj, qb) + ds * KB + lane * 16), acc[qb]);
    };
    waitvm<16>(); pull_wa(0); waitlds(); issue_wa(2, 0); issue_wa(2, 1);
    bar();
    mma_o(0);
    waitvm<8>(); pull_wa(1); waitlds(); issue_wa(3, 0); issue_wa(3, 1);
    bar();
    waitvm<8>(); pull_wa(2);
    bar();
    mma_o(1);
    bar();
    waitvm<0>(); pull_wa(3);
    bar();
    mma_o(2);
    bar();
    mma_o(3);
  }
  STAMP(12);
  constexpr int NTL = ROLE == 0 ? 1 : 3;
  const int tile0 = ROLE == 0 ? 12 + dt : 3 * dt;
  GAS unsigned* f2 = (GAS unsigned*)(P.ws + WS_F2 + (size_t)(b * 4 + i4) * 256 + w * 32);
  if (ROLE == 1 || w < 3) {
#pragma unroll
    for (int t = 0; t < NTL; ++t)
#pragma unroll
      for (int rg = 0; rg < 4; ++rg) {
        const u32x4 v = {__float_as_uint(acc[t][4 * rg]), __float_as_uint(acc[t][4 * rg + 1]), __float_as_uint(acc[t][4 * rg + 2]), __float_as_uint(acc[t][4 * rg + 3])};
        st_sc1(prs, ((((rg * 2 + hh) * 8 + g) * 15 + tile0 + t) * 32 + r) * 16, v);
      }
    waitvm<0>();
    if (lane == 0) __hip_atomic_store(f2 + g, tag, RLX_AGENT);
    STAMP(13);
    for (unsigned spin = 0; spin < SPIN_CAP; ++spin) {
      const unsigned v = __hip_atomic_load(f2 + (lane & 7), RLX_AGENT);
      if (__all(v == tag)) break;
      __builtin_amdgcn_s_sleep(1);
    }
    __builtin_amdgcn_fence(__ATOMIC_ACQUIRE, "wavefront");
    STAMP(14);
    u32x4 t[NTL][4];
#pragma unroll
    for (int tt = 0; tt < NTL; ++tt)
#pragma unroll
      for (int pp = 0; pp < 4; ++pp) t[tt][pp] = ld_sc1(prs, (((g * 8 + hh * 4 + pp) * 15 + tile0 + tt) * 32 + r) * 16);
#pragma unroll
    for (int tt = 0; tt < NTL; ++tt) {
      float s[4];
#pragma unroll
      for (int e = 0; e < 4; ++e)
        s[e] = xhalf_sum((__uint_as_float(t[tt][0][e]) + __uint_as_float(t[tt][1][e])) + (__uint_as_float(t[tt][2][e]) + __uint_as_float(t[tt][3][e])));
      const int nt = ROLE == 0 ? 4 : dt, qb = ROLE == 0 ? dt : tt;
      const u32x2 o = {pk2(s[0], s[1]), pk2(s[2], s[3])};
      if (hh == 0) *(u32x2*)(P.out + ((size_t)(b * SQ + 32 * qb + r) * DM + 160 * i4 + 32 * nt + 8 * (g >> 1) + 4 * (g & 1)) * 2) = o;
    }
    if (ROLE == 1 && w == 4 && g == 0 && i4 == 0 && lane == 0) __hip_atomic_store((GAS unsigned*)(P.ws + WS_EPOCH + b * 256), tag, RLX_AGENT);
  }
}

__global__ __launch_bounds__(512, 2) void k_fused(const char* __restrict__ x, const char* __restrict__ kc, const char* __restrict__ vc,
                                                   const char* __restrict__ wq, const char* __restrict__ wo, char* __restrict__ ws, char* __restrict__ out) {
  extern __shared__ __attribute__((aligned(16))) char smem[];
  const int g = blockIdx.x & 7, idx = blockIdx.x >> 3, b = idx >> 2, i4 = idx & 3, h = 4 * g + i4;
  Ptrs P;
  P.xb = x + (size_t)b * SQ * DM * 2;
  P.wqh = wq + (size_t)h * 256;
  P.kb = kc + ((size_t)(b * NH + h) * SK) * 256;
  P.vb = vc + ((size_t)(b * NH + h) * SK) * 256;
  P.wo = wo; P.ws = ws; P.out = out;
  const int rot = (b + 3 * i4 + g) % 10;
  const int w = __builtin_amdgcn_readfirstlane(threadIdx.x >> 6);
  if ((b & 1) == 0) { if (w < 4) body<0, 0>(smem, P, b, g, i4, rot, w); else body<0, 1>(smem, P, b, g, i4, rot, w); }
  else { if (w < 4) body<1, 0>(smem, P, b, g, i4, rot, w); else body<1, 1>(smem, P, b, g, i4, rot, w); }
}

extern "C" void kernel_launch(void* const* d_in, const int* in_sizes, int n_in, void* d_out, int out_size, void* d_ws, size_t ws_size,
                              hipStream_t stream) {
  (void)hipFuncSetAttribute((const void*)k_fused, hipFuncAttributeMaxDynamicSharedMemorySize, LDS_BYTES);
  hipLaunchKernelGGL(k_fused, dim3(256), dim3(512), LDS_BYTES, stream, (const char*)d_in[0], (const char*)d_in[1], (const char*)d_in[2],
                     (const char*)d_in[3], (const char*)d_in[4], (char*)d_ws, (char*)d_out);
}
```

```cpp
#include <hip/hip_runtime.h>
#include <hip/hip_bf16.h>

typedef __attribute__((ext_vector_type(8))) __bf16 bf16x8;
typedef __attribute__((ext_vector_type(4))) __bf16 bf16x4;
typedef __attribute__((ext_vector_type(2))) __bf16 bf16x2;
typedef __attribute__((ext_vector_type(16))) float f32x16;
typedef __attribute__((ext_vector_type(4))) float f32x4;
typedef __attribute__((ext_vector_type(2))) float f32x2;
typedef __attribute__((ext_vector_type(4))) unsigned u32x4;
typedef __attribute__((ext_vector_type(2))) unsigned u32x2;
#define LAS __attribute__((address_space(3)))
#define GAS __attribute__((address_space(1)))
#define DI __device__ __forceinline__
#define AI __attribute__((always_inline))
#define MFMA(a, b, c) __builtin_amdgcn_mfma_f32_32x32x16_bf16((a), (b), (c), 0, 0, 0)
#define SB() __builtin_amdgcn_sched_barrier(0)
#define NOUNROLL _Pragma("clang loop unroll(disable)")

#define STAMP(i) do {} while (0)

constexpr int SQ = 96, DM = 640, NH = 32, SK = 512;
constexpr unsigned KB = 1024;
constexpr unsigned LDS_BYTES = 163840;

constexpr size_t WS_EPOCH = 0;
constexpr size_t WS_F1 = 4096;
constexpr size_t WS_F2 = 16384;
constexpr size_t WS_O = 1u << 20;
constexpr size_t WS_P = 8u << 20;
constexpr unsigned PBYTES = 8 * 8 * 3 * 2 * 1024;

#define DMA_PRE "s_mov_b32 m0, %0\n\ts_nop 0\n\t"
#define DMA4(SUF) DMA_PRE "global_load_lds_dwordx4 %2, %1" SUF "\n\tglobal_load_lds_dwordx4 %3, %1 offset:1024" SUF \
                  "\n\tglobal_load_lds_dwordx4 %4, %1 offset:2048" SUF "\n\tglobal_load_lds_dwordx4 %5, %1 offset:3072" SUF
template <int POL> DI void dma4(unsigned v0, unsigned v1, unsigned v2, unsigned v3, const void* sbase, unsigned lds) {
  lds = __builtin_amdgcn_readfirstlane(lds);
  if constexpr (POL == 1) asm volatile(DMA4(" nt") :: "s"(lds), "s"(sbase), "v"(v0), "v"(v1), "v"(v2), "v"(v3) : "memory");
  else if constexpr (POL == 2) asm volatile(DMA4(" sc1") :: "s"(lds), "s"(sbase), "v"(v0), "v"(v1), "v"(v2), "v"(v3) : "memory");
  else asm volatile(DMA4("") :: "s"(lds), "s"(sbase), "v"(v0), "v"(v1), "v"(v2), "v"(v3) : "memory");
}
DI void dma3(unsigned v0, unsigned v1, unsigned v2, const void* sbase, unsigned lds) {
  lds = __builtin_amdgcn_readfirstlane(lds);
  asm volatile(DMA_PRE "global_load_lds_dwordx4 %2, %1\n\tglobal_load_lds_dwordx4 %3, %1 offset:1024\n\tglobal_load_lds_dwordx4 %4, %1 offset:2048"
               :: "s"(lds), "s"(sbase), "v"(v0), "v"(v1), "v"(v2) : "memory");
}
DI void dma2nt(unsigned v0, const void* sbase, unsigned lds) {
  lds = __builtin_amdgcn_readfirstlane(lds);
  asm volatile(DMA_PRE "global_load_lds_dwordx4 %2, %1 nt\n\tglobal_load_lds_dwordx4 %2, %1 offset:1024 nt" :: "s"(lds), "s"(sbase), "v"(v0) : "memory");
}
template <int N> DI void waitvm() { asm volatile("s_waitcnt vmcnt(%0)" ::"i"(N) : "memory"); }
DI void bar() { asm volatile("s_waitcnt lgkmcnt(0)\n\ts_barrier" ::: "memory"); }
DI void waitlds() { asm volatile("s_waitcnt lgkmcnt(0)" ::: "memory"); }
DI unsigned pk2(float a, float b) { f32x2 t = {a, b}; bf16x2 p = __builtin_convertvector(t, bf16x2); return __builtin_bit_cast(unsigned, p); }
DI bf16x8 lds128(unsigned a) { return *(const LAS bf16x8*)(size_t)a; }
DI void sts128(unsigned a, u32x4 v) { *(LAS u32x4*)(size_t)a = v; }
DI bf16x8 ldstr(unsigned a0, unsigned a1) {
  bf16x4 a = __builtin_amdgcn_ds_read_tr16_b64_v4bf16((LAS bf16x4*)(size_t)a0);
  bf16x4 b = __builtin_amdgcn_ds_read_tr16_b64_v4bf16((LAS bf16x4*)(size_t)a1);
  return __builtin_shufflevector(a, b, 0, 1, 2, 3, 4, 5, 6, 7);
}
DI float xhalf_max(float v) { auto r = __builtin_amdgcn_permlane32_swap(__float_as_uint(v), __float_as_uint(v), false, false); return fmaxf(__uint_as_float(r[0]), __uint_as_float(r[1])); }
DI float xhalf_sum(float v) { auto r = __builtin_amdgcn_permlane32_swap(__float_as_uint(v), __float_as_uint(v), false, false); return __uint_as_float(r[0]) + __uint_as_float(r[1]); }
DI u32x4 pack8(const f32x16& a, int s) {
  u32x4 o = {pk2(a[8 * s], a[8 * s + 1]), pk2(a[8 * s + 2], a[8 * s + 3]), pk2(a[8 * s + 4], a[8 * s + 5]), pk2(a[8 * s + 6], a[8 * s + 7])};
  return o;
}
DI void pin(u32x4& x) { asm volatile("" : "+v"(x)); }
template <int NM, int ND> DI void ilv() {
#pragma unroll
  for (int i = 0; i < NM; ++i) { __builtin_amdgcn_sched_group_barrier(0x008, 1, 0); __builtin_amdgcn_sched_group_barrier(0x100, ND, 0); }
  __builtin_amdgcn_sched_barrier(0);
}
DI void st_sc1(__amdgpu_buffer_rsrc_t rs, unsigned off, u32x4 v) { __builtin_amdgcn_raw_buffer_store_b128(v, rs, off, 0, 16); }
DI u32x4 ld_sc1(__amdgpu_buffer_rsrc_t rs, unsigned off) { return __builtin_amdgcn_raw_buffer_load_b128(rs, off, 0, 16); }
#define RLX_AGENT __ATOMIC_RELAXED, __HIP_MEMORY_SCOPE_AGENT
constexpr unsigned SPIN_CAP = 1u << 18;

constexpr unsigned S(int i) { return 8 * KB * i; }
constexpr unsigned area(int a) { return a == 0 ? S(12) : a == 1 ? S(16) : a == 2 ? S(8) : a == 3 ? S(0) : S(4); }
constexpr unsigned AREA_TAB = 0x10243u;
constexpr unsigned QIMG = S(8), PB = S(8), PA = S(17), MISC = S(11);
constexpr unsigned L_MX = MISC, L_SM = MISC + 3072, L_TAG = MISC + 6144, L_CNT = MISC + 6400;
constexpr unsigned vslot(int j) { return j < 5 ? S(12 + j) : j < 13 ? S(j - 5) : S(12 + j - 13); }
constexpr unsigned oimg(int jj) { return jj == 0 ? S(12) : jj == 1 ? S(8) : jj == 2 ? S(0) : S(4); }
constexpr unsigned OIMG_TAB = 0x408Cu;
constexpr unsigned wobs(int jj) { return jj == 0 ? S(15) : S(16 + jj); }
constexpr unsigned wset(int set, int t) { return set == 0 ? (t == 0 ? S(16) : S(t - 1)) : S(3 + t); }
enum { C_O0 = 0, C_FREEX = 4, C_FREEY = 5, C_WOB = 6, C_REL0 = 7, C_L01 = 11, C_P01 = 12, C_L2 = 13, C_L3 = 14 };
DI void cnt_add(unsigned idx, unsigned n = 1) {
  unsigned long long keep; const unsigned a = L_CNT + idx * 4;
  asm volatile("s_mov_b64 %0, exec\n\ts_mov_b64 exec, 1\n\tds_add_u32 %1, %2\n\ts_mov_b64 exec, %0" : "=&s"(keep) : "v"(a), "v"(n) : "memory");
}
DI void cnt_wait(unsigned idx) {
  const unsigned a = L_CNT + idx * 4; unsigned v, s, n = 1u << 24;
  asm volatile("1:\n\tds_read_b32 %0, %3\n\ts_waitcnt lgkmcnt(0)\n\tv_readfirstlane_b32 %1, %0\n\ts_cmp_ge_u32 %1, 4\n\ts_cbranch_scc1 2f\n\t"
               "s_sleep 1\n\ts_sub_u32 %2, %2, 1\n\ts_cmp_lg_u32 %2, 0\n\ts_cbranch_scc1 1b\n2:"
               : "=&v"(v), "=&s"(s), "+s"(n) : "v"(a) : "memory", "scc");
}

__global__ __launch_bounds__(512, 2) void k_fused(const char* __restrict__ x, const char* __restrict__ kc, const char* __restrict__ vc,
                                                   const char* __restrict__ wq, const char* __restrict__ wo, char* __restrict__ ws, char* __restrict__ out) {
  const int g = blockIdx.x & 7, idx = blockIdx.x >> 3, b = idx >> 2, i4 = idx & 3, h = 4 * g + i4;
  const int c4 = (i4 + (b >> 1)) & 3;
  const bool tA = (b & 1) == 0;
  const int tid = threadIdx.x, lane = tid & 63;
  const int w = __builtin_amdgcn_readfirstlane(tid >> 6), dt = w & 3;
  const bool r1 = w >= 4;
  const int r = lane & 31, hh = lane >> 5;
  const int blk = (lane >> 4) & 1, q4 = (lane & 15) >> 2, p4 = lane & 3;
  const unsigned u = lane >> 4, l16 = lane * 16;
  STAMP(0);
  const char* kb = kc + ((size_t)(b * NH + h) * SK) * 256;
  const char* vb = vc + ((size_t)(b * NH + h) * SK) * 256;
  const unsigned vo_tr = u * 256 + (((lane & 15) ^ (u << 2)) * 16);
  const unsigned vqv = vo_tr + dt * 8192, vtv = vo_tr + dt * 2048;
  const unsigned k0v = u * 256 + (((lane & 15) ^ u) * 16) + dt * 8192, k1v = k0v ^ 64, k2v = k0v ^ 128, k3v = k0v ^ 192;
  auto karea = [&](int q) AI { return (q < 2 && tA) ? area(q) : area(q + 1); };
  auto issue_kq = [&](int q) AI {
    const unsigned d = karea(q) + dt * 8 * KB;
    dma4<1>(k0v, k1v, k2v, k3v, kb + q * 32768, d);
    dma4<1>(k0v, k1v, k2v, k3v, kb + q * 32768 + 4096, d + 4096);
  };
  auto issue_v = [&](int j) AI { dma2nt(vtv, vb + j * 8192, vslot(j) + dt * 2 * KB); };
  auto issue_vh = [&](int j0, int half) AI { dma4<1>(vqv, vqv, vqv, vqv, vb + j0 * 8192 + half * 4096, vslot(j0) + dt * 8 * KB + half * 4096); };
  bf16x8 kf[2][8];
  const unsigned kfo = dt * 8 * KB + r * 256 + ((hh ^ (r & 15)) * 16);
  auto pull_k = [&](int kt) AI {
    const unsigned slot = (r1 ? area(3 + kt) : karea(kt)) + kfo;
#pragma unroll
    for (int s = 0; s < 8; ++s) kf[kt][s] = lds128(slot ^ (s * 32));
  };

  if (!r1) {
    const unsigned epoch = __hip_atomic_load((GAS unsigned*)(ws + WS_EPOCH + (b * 8 + g) * 64), RLX_AGENT);
    if (!tA) { bar(); pull_k(0); bar(); pull_k(1); }
    STAMP(1);
    f32x16 qacc[3] = {};
    const int p4s = ((p4 & 1) << 1) | (p4 >> 1);
    const unsigned a_off = (8 * hh + q4) * 256 + (((4 * dt + 2 * blk + (p4s >> 1)) ^ (q4 << 2)) * 16) + 8 * (p4s & 1);
    const unsigned x_off = r * 128 + 16 * KB + ((hh ^ ((r >> 1) & 7)) * 16);
    bf16x8 fa[4] = {}, fb[4][3] = {};
    auto read_q = [&](unsigned base, int ks) AI {
      fa[ks] = ldstr(base + a_off + ks * 4096, base + a_off + ks * 4096 + 1024);
#pragma unroll
      for (int j = 0; j < 3; ++j) fb[ks][j] = lds128(base + (x_off ^ (ks * 32)) + j * 4096);
    };
    auto mma_q = [&](int ks) AI {
#pragma unroll
      for (int j = 0; j < 3; ++j) qacc[j] = MFMA(fa[ks], fb[ks][j], qacc[j]);
    };
    NOUNROLL
    for (int s0 = 0; s0 < 10; ++s0) {
      const unsigned base = ((AREA_TAB >> (4 * (s0 >= 5 ? s0 - 5 : s0))) & 15u) << 15;
      bar(); SB();
      read_q(base, 0); read_q(base, 1); mma_q(2); ilv<3, 4>();
      read_q(base, 2); mma_q(3); ilv<3, 2>();
      read_q(base, 3); mma_q(0); ilv<3, 2>();
      mma_q(1); SB();
    }
    mma_q(2); mma_q(3);
#pragma unroll
    for (int j = 0; j < 3; ++j)
#pragma unroll
      for (int sx = 0; sx < 2; ++sx) sts128(QIMG + (j * 8 + sx) * KB + dt * 2 * KB + l16, pack8(qacc[j], sx));
    if (tid < 16) *(LAS unsigned*)(size_t)(L_CNT + tid * 4) = 0u;
    if (tid == 0) *(LAS unsigned*)(size_t)L_TAG = epoch * 1664525u + 1013904223u;
    bar();
    STAMP(2);
  } else {
    const char* xb = x + (size_t)b * SQ * DM * 2;
    const char* wqh = wq + (size_t)h * 256;
    const unsigned vo_x = (lane >> 3) * 1280 + (((lane & 7) ^ u) * 16);
    const unsigned wq0 = u * 8192 + (((lane & 15) ^ (u << 2)) * 16) + dt * 16 * 8192, wq1 = wq0 + (32768 - 1024), wq2 = wq0 + 2 * (32768 - 1024), wq3 = wq0 + 3 * (32768 - 1024);
    const unsigned xb0 = (vo_x ^ ((dt & 1) << 6)) + dt * 3 * 10240, xb1 = (vo_x ^ (((dt + 1) & 1) << 6)) + dt * 3 * 10240 + (10240 - 1024), xb2 = xb0 + 2 * (10240 - 1024);
    const int rot = (b + 3 * i4 + g) % 10;
    auto issue_q = [&](int s0) AI {
      int s = s0 + rot; if (s >= 10) s -= 10;
      const unsigned base = ((AREA_TAB >> (4 * (s0 >= 5 ? s0 - 5 : s0))) & 15u) << 15;
      dma4<0>(wq0, wq1, wq2, wq3, wqh + (size_t)s * (64 * 8192), base + 4 * dt * KB);
      dma3(xb0, xb1, xb2, xb + s * 128, base + 16 * KB + 3 * dt * KB);
    };
    if (tA) {
      issue_q(0); issue_q(1); issue_q(2); waitvm<14>(); bar();
    } else {
      issue_kq(0); issue_kq(1); issue_kq(2);
      waitvm<16>(); bar(); issue_kq(3);
      waitvm<16>(); bar(); issue_q(0);
      waitvm<15>(); pull_k(0); issue_q(1);
      waitvm<14>(); pull_k(1);
      STAMP(1);
      waitvm<7>(); bar(); issue_q(2);
    }
    NOUNROLL
    for (int s0 = 0; s0 < 7; ++s0) { issue_q(s0 + 3); waitvm<14>(); bar(); }
    if (tA) {
      issue_kq(0); waitvm<15>(); bar();
      issue_kq(1); waitvm<16>(); bar();
      issue_kq(2); waitvm<16>(); bar();
      issue_kq(3); waitvm<16>(); bar();
    } else {
      waitvm<7>(); bar();
      issue_vh(0, 0); issue_vh(0, 1); waitvm<8>(); bar();
      issue_v(4); bar();
      issue_vh(5, 0); issue_vh(5, 1);
    }
    STAMP(2);
    bar();
  }

  f32x16 sacc[2][3] = {};
  {
    bf16x8 qf[2][6];
    auto rd_q6 = [&](int st) AI {
#pragma unroll
      for (int i = 0; i < 6; ++i) qf[st & 1][i] = lds128(QIMG + ((i % 3) * 8 + 2 * st + i / 3) * KB + l16);
    };
    auto s_tile = [&](int kt) AI {
      rd_q6(0); SB();
#pragma unroll
      for (int st = 0; st < 4; ++st) {
        if (st < 3) rd_q6(st + 1);
#pragma unroll
        for (int i = 0; i < 6; ++i) sacc[kt][i % 3] = MFMA(kf[kt][2 * st + i / 3], qf[st & 1][i], sacc[kt][i % 3]);
        ilv<6, 1>();
      }
    };
    if (tA) {
      if (r1) waitvm<8>(); pull_k(0); if (r1) issue_vh(0, 0);
      s_tile(0);
      if (r1) { issue_vh(0, 1); waitvm<8>(); } else bar(); pull_k(1); if (r1) issue_v(4);
      s_tile(1);
    } else {
      rd_q6(0); SB();
#pragma unroll
      for (int st = 0; st < 4; ++st) {
        if (st < 3) rd_q6(st + 1);
#pragma unroll
        for (int i = 0; i < 6; ++i) {
          sacc[0][i % 3] = MFMA(kf[0][2 * st + i / 3], qf[st & 1][i], sacc[0][i % 3]);
          sacc[1][i % 3] = MFMA(kf[1][2 * st + i / 3], qf[st & 1][i], sacc[1][i % 3]);
        }
        ilv<6, 1>();
      }
    }
  }
  const unsigned mxo = r * 32;
#pragma unroll
  for (int qb = 0; qb < 3; ++qb) {
    float m = sacc[0][qb][0];
#pragma unroll
    for (int kt = 0; kt < 2; ++kt)
#pragma unroll
      for (int i = 0; i < 16; ++i) m = fmaxf(m, sacc[kt][qb][i]);
    m = xhalf_max(m);
    if (hh == 0) *(LAS float*)(size_t)(L_MX + qb * 1024 + mxo + w * 4) = m;
  }
  if (r1) {
    if (tA) { issue_vh(5, 0); issue_vh(5, 1); waitvm<10>(); } else { issue_vh(9, 0); issue_vh(9, 1); waitvm<18>(); }
  }
  bar();
  STAMP(3);
  const float cs = 0.08838834764831845f * 1.4426950408889634f;
  auto max4 = [&](int qb, int half) AI {
    f32x4 m = *(const LAS f32x4*)(size_t)(L_MX + qb * 1024 + mxo + half * 16);
    return fmaxf(fmaxf(m[0], m[1]), fmaxf(m[2], m[3]));
  };
  float m0[3], lsum[3] = {0.f, 0.f, 0.f};
#pragma unroll
  for (int qb = 0; qb < 3; ++qb) m0[qb] = fmaxf(max4(qb, 0), max4(qb, r1));
  u32x4 pr[3][2];
  auto exp_tile = [&](int kt) AI {
#pragma unroll
    for (int qb = 0; qb < 3; ++qb) {
      const float nm = -m0[qb] * cs;
#pragma unroll
      for (int i = 0; i < 18; ++i) {
        if (i < 16) sacc[kt][qb][i] = __builtin_amdgcn_exp2f(fmaf(sacc[kt][qb][i], cs, nm));
        if (i >= 2) asm volatile("v_add_f32 %0, %0, %1" : "+v"(lsum[qb]) : "v"(sacc[kt][qb][i - 2]));
        if (i >= 2 && (i & 1)) pr[qb][(i - 2) >> 3][((i - 2) >> 1) & 3] = pk2(sacc[kt][qb][i - 3], sacc[kt][qb][i - 2]);
        SB();
      }
      pin(pr[qb][0]); pin(pr[qb][1]);
    }
  };
  auto put_p = [&](int kt) AI {
#pragma unroll
    for (int qb = 0; qb < 3; ++qb)
#pragma unroll
      for (int sx = 0; sx < 2; ++sx) sts128((kt == 0 ? PA : PB) + dt * 6 * KB + l16 + (sx * 3 + qb) * KB, pr[qb][sx]);
  };
  const char* wob = wo + (size_t)(4 * g) * 128 * 1280 + c4 * 320;
  const unsigned wa0 = u * 1280 + (((lane & 15) ^ (u << 2)) * 16) + dt * 32 * 1280, wa1 = wa0 + (5120 - 1024), wa2 = wa0 + 2 * (5120 - 1024), wa3 = wa0 + 3 * (5120 - 1024);
  auto issue_wa = [&](int jj) AI {
    const char* src = wob + (size_t)(((i4 + jj) & 3) * 128) * 1280;
    const unsigned d = (jj & 1) ? S(3) + dt * 8 * KB : (dt == 0 ? S(16) : (dt - 1) * 8 * KB);
    dma4<0>(wa0, wa1, wa2, wa3, src, d);
    dma4<0>(wa0, wa1, wa2, wa3, src + 16 * 1280, d + 4096);
  };
  if (r1 && tA) issue_vh(9, 0);
  exp_tile(0);
  if (r1) {
    if (tA) issue_vh(9, 1);
    cnt_wait(C_REL0); issue_v(13); issue_v(14); issue_v(15);
    waitvm<14>();
    bar();
  }
  put_p(0);
  exp_tile(1);
#pragma unroll
  for (int qb = 0; qb < 3; ++qb) {
    const float s = xhalf_sum(lsum[qb]);
    if (hh == 0) *(LAS float*)(size_t)(L_SM + qb * 1024 + mxo + w * 4) = s;
  }
  if (r1) { cnt_wait(C_REL0 + 1); issue_wa(0); waitvm<14>(); }
  STAMP(4);
  bar();
  STAMP(5);
  put_p(1);

  const unsigned v_off = (4 * hh + q4) * 256 + (((4 * dt + 2 * blk + (p4 >> 1)) ^ (q4 << 2)) * 16) + 8 * (p4 & 1);
  const unsigned tag = __builtin_amdgcn_readfirstlane(*(const LAS unsigned*)(size_t)L_TAG);
  f32x16 acc[3] = {};
  if (!r1) {
    f32x16 oa[2][3] = {};
    bf16x8 vf[4][2], pf[2][2][3];
    auto rd_v = [&](int j) AI {
#pragma unroll
      for (int sx = 0; sx < 2; ++sx) vf[j & 3][sx] = ldstr(vslot(j) + v_off + sx * 4096, vslot(j) + v_off + sx * 4096 + 2048);
    };
    auto rd_p = [&](int j) AI {
#pragma unroll
      for (int sx = 0; sx < 2; ++sx)
#pragma unroll
        for (int qb = 0; qb < 3; ++qb) pf[j & 1][sx][qb] = lds128((((j >> 2) & 1) ? PB : PA) + ((j & 3) * 6 + sx * 3 + qb) * KB + l16);
    };
    auto mma_pv = [&](int j) AI {
#pragma unroll
      for (int sx = 0; sx < 2; ++sx)
#pragma unroll
        for (int qb = 0; qb < 3; ++qb) oa[j >> 3][qb] = MFMA(vf[j & 3][sx], pf[j & 1][sx][qb], oa[j >> 3][qb]);
    };
    auto pv_quarter = [&](int q) AI {
      rd_v(4 * q); rd_p(4 * q); rd_v(4 * q + 1); rd_v(4 * q + 2);
      if (q > 0) { mma_pv(4 * q - 1); ilv<6, 3>(); } else SB();
      rd_v(4 * q + 3); rd_p(4 * q + 1); mma_pv(4 * q); ilv<6, 2>();
      rd_p(4 * q + 2); mma_pv(4 * q + 1); ilv<6, 1>();
      asm volatile("s_waitcnt lgkmcnt(6)" ::: "memory"); cnt_add(C_REL0 + q); SB();
      rd_p(4 * q + 3); mma_pv(4 * q + 2); ilv<6, 1>();
    };
    pv_quarter(0); bar(); SB();
    pv_quarter(1); bar(); SB();
    STAMP(6);
    pv_quarter(2);
    float sc0[3], sc1[3];
#pragma unroll
    for (int qb = 0; qb < 3; ++qb) {
      const float m = fmaxf(m0[qb], max4(qb, 1));
      const float al = __builtin_amdgcn_exp2f((m0[qb] - m) * cs);
      f32x4 l0 = *(const LAS f32x4*)(size_t)(L_SM + qb * 1024 + mxo);
      f32x4 l1 = *(const LAS f32x4*)(size_t)(L_SM + qb * 1024 + mxo + 16);
      const float l = al * ((l0[0] + l0[1]) + (l0[2] + l0[3])) + ((l1[0] + l1[1]) + (l1[2] + l1[3]));
      sc1[qb] = 1.0f / l; sc0[qb] = al * sc1[qb];
    }
    bar(); SB();
    STAMP(7);
    pv_quarter(3);
#pragma unroll
    for (int qb = 0; qb < 3; ++qb)
#pragma unroll
      for (int i = 0; i < 16; ++i) oa[0][qb][i] *= sc0[qb];
    mma_pv(15);
    const char* obase = ws + WS_O + (size_t)(b * NH + 4 * g) * 24576;
    const __amdgpu_buffer_rsrc_t ors = __builtin_amdgcn_make_buffer_rsrc((char*)obase, 0, 4 * 24576, 0x00020000);
    cnt_wait(C_REL0 + 3);
#pragma unroll
    for (int qb = 0; qb < 3; ++qb) {
#pragma unroll
      for (int i = 0; i < 16; ++i) oa[1][qb][i] = fmaf(oa[1][qb][i], sc1[qb], oa[0][qb][i]);
#pragma unroll
      for (int sx = 0; sx < 2; ++sx) {
        const u32x4 o = pack8(oa[1][qb], sx);
        st_sc1(ors, i4 * 24576 + (qb * 8 + sx) * KB + dt * 2 * KB + l16, o);
        sts128(oimg(0) + (qb * 8 + sx) * KB + dt * 2 * KB + l16, o);
      }
    }
    waitlds(); cnt_add(C_O0);
    STAMP(8);
    waitvm<0>();
    GAS unsigned* fl = (GAS unsigned*)(ws + WS_F1 + (size_t)(b * 8 + g) * 64);
    if (lane == 0) __hip_atomic_store(fl + dt * 4 + i4, tag, RLX_AGENT);
    STAMP(9);
    if (w == 3) {
      unsigned pend = 0xEu;
      cnt_wait(C_O0);
      NOUNROLL
      for (unsigned spin = 0; pend && spin < SPIN_CAP; ++spin) {
        const unsigned fv = __hip_atomic_load(fl + (lane & 12) + ((i4 + lane) & 3), RLX_AGENT);
        const unsigned m = (unsigned)__ballot(fv == tag);
        const unsigned rdy = m & (m >> 4) & (m >> 8) & (m >> 12) & pend;
        if (!rdy) { __builtin_amdgcn_s_sleep(1); continue; }
        NOUNROLL
        for (unsigned t = rdy; t; t &= t - 1) {
          const unsigned jj = __builtin_ctz(t);
          if (jj >= 2) cnt_wait(C_FREEX + jj - 2);
          const char* src = obase + ((i4 + jj) & 3) * 24576;
          const unsigned d = ((OIMG_TAB >> (4 * jj)) & 15u) << 13;
          NOUNROLL
          for (int i = 0; i < 6; ++i) dma4<2>(l16, l16, l16, l16, src + i * 4096, d + i * 4096);
        }
        waitvm<0>();
        NOUNROLL
        for (unsigned t = rdy; t; t &= t - 1) cnt_add(__builtin_ctz(t), 4);
        pend &= ~rdy;
      }
      if (i4 == 0 && lane == 0) __hip_atomic_store((GAS unsigned*)(ws + WS_EPOCH + (b * 8 + g) * 64), tag, RLX_AGENT);
    } else {
      const unsigned w_off = (4 * hh + q4) * 64 + (2 * blk + (p4 >> 1)) * 16 + 8 * (p4 & 1);
      cnt_wait(C_WOB);
      NOUNROLL
      for (int jj = 0; jj < 4; ++jj) {
        cnt_wait(jj);
        const unsigned wb = (jj == 0 ? S(15) : S(16) + jj * 8 * KB) + w_off;
        const unsigned ob = (((OIMG_TAB >> (4 * jj)) & 15u) << 13) + dt * 8 * KB + l16;
        bf16x8 af[8], of[8];
#pragma unroll
        for (int ds = 0; ds < 8; ++ds) { af[ds] = ldstr(wb + ds * KB, wb + ds * KB + 512); of[ds] = lds128(ob + ds * KB); }
#pragma unroll
        for (int ds = 0; ds < 8; ++ds) acc[0] = MFMA(af[ds], of[ds], acc[0]);
      }
    }
  } else {
    bf16x8 wf[4][8];
    auto pull_wa = [&](int jj) AI {
#pragma unroll
      for (int ds = 0; ds < 8; ++ds) { const unsigned a = wset(jj & 1, ds >> 1) + v_off + (ds & 1) * 4096; wf[jj][ds] = ldstr(a, a + 2048); }
    };
    auto mma_o = [&](int jj) AI {
      bf16x8 of[3][3];
#pragma unroll
      for (int ds = 0; ds < 2; ++ds)
#pragma unroll
        for (int qb = 0; qb < 3; ++qb) of[ds][qb] = lds128(oimg(jj) + (qb * 8 + ds) * KB + l16);
      SB();
#pragma unroll
      for (int ds = 0; ds < 8; ++ds) {
        if (ds < 6) {
#pragma unroll
          for (int qb = 0; qb < 3; ++qb) of[(ds + 2) % 3][qb] = lds128(oimg(jj) + (qb * 8 + ds + 2) * KB + l16);
        }
#pragma unroll
        for (int qb = 0; qb < 3; ++qb) acc[qb] = MFMA(wf[jj][ds], of[ds % 3][qb], acc[qb]);
        ilv<3, 1>();
      }
    };
    cnt_wait(C_REL0 + 2); issue_wa(1);
    waitvm<16>();
    bar();
    STAMP(6);
    {
      const unsigned wb0 = (lane >> 2) * 1280 + (lane & 3) * 16 + 256, wb1 = wb0 + (20480 - 1024), wb2 = wb0 + 2 * (20480 - 1024), wb3 = wb0 + 3 * (20480 - 1024);
      const char* src = wob + (size_t)(((i4 + dt) & 3) * 128) * 1280;
      const unsigned d = dt == 0 ? S(15) : S(16) + dt * 8 * KB;
      dma4<0>(wb0, wb1, wb2, wb3, src, d);
      dma4<0>(wb0, wb1, wb2, wb3, src + 64 * 1280, d + 4096);
    }
    waitvm<8>(); cnt_add(C_L01); cnt_wait(C_L01);
    pull_wa(0); pull_wa(1); waitlds(); cnt_add(C_P01); cnt_wait(C_P01);
    STAMP(7);
    issue_wa(2); issue_wa(3);
    waitvm<16>(); cnt_add(C_WOB);
    waitvm<8>(); cnt_add(C_L2); cnt_wait(C_L2); pull_wa(2); waitlds(); cnt_add(C_FREEX);
    waitvm<0>(); cnt_add(C_L3); cnt_wait(C_L3); pull_wa(3); waitlds(); cnt_add(C_FREEY);
    STAMP(8);
    cnt_wait(0); mma_o(0);
    STAMP(9);
    cnt_wait(1); mma_o(1);
    STAMP(10);
    cnt_wait(2); mma_o(2);
    STAMP(11);
    cnt_wait(3); mma_o(3);
  }
  STAMP(12);
  if (w != 3) {
    char* pb = ws + WS_P + (size_t)(b * 4 + c4) * PBYTES + (((r >> 2) * 8 + w) * 6) * KB + (g * 8 + (r & 3) + 4 * hh) * 16;
#pragma unroll
    for (int t = 0; t < 3; ++t) if (t == 0 || r1) {
#pragma unroll
      for (int uu = 0; uu < 2; ++uu) *(u32x4*)(pb + (t * 2 + uu) * KB) = pack8(acc[t], uu);
    }
  }
}

__global__ __launch_bounds__(512) void k_reduce(const char* __restrict__ ws, char* __restrict__ out) {
  const int g = blockIdx.x & 7, idx = blockIdx.x >> 3, b = idx >> 2, c4 = idx & 3;
  const int lane = threadIdx.x & 63, w = __builtin_amdgcn_readfirstlane(threadIdx.x >> 6), dt = w & 3;
  const bool r1 = w >= 4;
  if (w == 3) return;
  const char* pb = ws + WS_P + (size_t)(b * 4 + c4) * PBYTES + ((g * 8 + w) * 6) * KB + lane * 16;
  u32x4 t[3][2];
#pragma unroll
  for (int tt = 0; tt < 3; ++tt)
#pragma unroll
    for (int uu = 0; uu < 2; ++uu) t[tt][uu] = *(const u32x4*)(pb + ((r1 ? tt : 0) * 2 + uu) * KB);
  char* orow = out + ((size_t)(b * SQ + (r1 ? 0 : 32 * dt) + 4 * g + (lane & 3)) * DM + 160 * c4 + (r1 ? 32 * dt : 128) + 8 * (lane >> 4) + (lane & 4)) * 2;
#pragma unroll
  for (int tt = 0; tt < 3; ++tt) if (tt == 0 || r1) {
    float xx[16], y[8], z[4];
#pragma unroll
    for (int i = 0; i < 8; ++i) { const unsigned d = t[tt][i >> 2][i & 3]; xx[2 * i] = __uint_as_float(d << 16); xx[2 * i + 1] = __uint_as_float(d & 0xffff0000u); }
#pragma unroll
    for (int i = 0; i < 8; ++i) { auto q = __builtin_amdgcn_permlane32_swap(__float_as_uint(xx[i]), __float_as_uint(xx[i + 8]), false, false); y[i] = __uint_as_float(q[0]) + __uint_as_float(q[1]); }
#pragma unroll
    for (int i = 0; i < 4; ++i) { auto q = __builtin_amdgcn_permlane16_swap(__float_as_uint(y[i]), __float_as_uint(y[i + 4]), false, false); z[i] = __uint_as_float(q[0]) + __uint_as_float(q[1]); }
#pragma unroll
    for (int i = 0; i < 4; ++i) z[i] += __uint_as_float(__builtin_amdgcn_update_dpp(0u, __float_as_uint(z[i]), 0x128, 0xf, 0xf, false));
    const u32x2 o = {pk2(z[0], z[1]), pk2(z[2], z[3])};
    if ((lane & 8) == 0) *(u32x2*)(orow + (size_t)tt * 32 * DM * 2) = o;
  }
}

extern "C" void kernel_launch(void* const* d_in, const int* in_sizes, int n_in, void* d_out, int out_size, void* d_ws, size_t ws_size,
                              hipStream_t stream) {
  (void)hipFuncSetAttribute((const void*)k_fused, hipFuncAttributeMaxDynamicSharedMemorySize, LDS_BYTES);
  hipLaunchKernelGGL(k_fused, dim3(256), dim3(512), LDS_BYTES, stream, (const char*)d_in[0], (const char*)d_in[1], (const char*)d_in[2],
                     (const char*)d_in[3], (const char*)d_in[4], (char*)d_ws, (char*)d_out);
  hipLaunchKernelGGL(k_reduce, dim3(256), dim3(512), 0, stream, (const char*)d_ws, (char*)d_out);
}
```

```cpp
#include <hip/hip_runtime.h>
#include <hip/hip_bf16.h>

typedef __attribute__((ext_vector_type(8))) __bf16 bf16x8;
typedef __attribute__((ext_vector_type(4))) __bf16 bf16x4;
typedef __attribute__((ext_vector_type(2))) __bf16 bf16x2;
typedef __attribute__((ext_vector_type(16))) float f32x16;
typedef __attribute__((ext_vector_type(4))) float f32x4;
typedef __attribute__((ext_vector_type(2))) float f32x2;
typedef __attribute__((ext_vector_type(4))) unsigned u32x4;
typedef __attribute__((ext_vector_type(2))) unsigned u32x2;
#define LAS __attribute__((address_space(3)))
#define GAS __attribute__((address_space(1)))
#define DI __device__ __forceinline__
#define AI __attribute__((always_inline))
#define MFMA(a, b, c) __builtin_amdgcn_mfma_f32_32x32x16_bf16((a), (b), (c), 0, 0, 0)
#define SB() __builtin_amdgcn_sched_barrier(0)
#define NOUNROLL _Pragma("clang loop unroll(disable)")

#define STAMP(i) do {} while (0)

constexpr int SQ = 96, DM = 640, NH = 32, SK = 512;
constexpr unsigned KB = 1024;
constexpr unsigned LDS_BYTES = 163840;

constexpr size_t WS_EPOCH = 0;
constexpr size_t WS_F1 = 4096;
constexpr size_t WS_F2 = 16384;
constexpr size_t WS_O = 1u << 20;
constexpr size_t WS_P = 8u << 20;
constexpr unsigned PBYTES = 8 * 8 * 3 * 2 * 1024;

#define DMA_PRE "s_mov_b32 m0, %0\n\ts_nop 0\n\t"
#define DMA4(SUF) DMA_PRE "global_load_lds_dwordx4 %2, %1" SUF "\n\tglobal_load_lds_dwordx4 %3, %1 offset:1024" SUF \
                  "\n\tglobal_load_lds_dwordx4 %4, %1 offset:2048" SUF "\n\tglobal_load_lds_dwordx4 %5, %1 offset:3072" SUF
template <int POL> DI void dma4(unsigned v0, unsigned v1, unsigned v2, unsigned v3, const void* sbase, unsigned lds) {
  lds = __builtin_amdgcn_readfirstlane(lds);
  if constexpr (POL == 1) asm volatile(DMA4(" nt") :: "s"(lds), "s"(sbase), "v"(v0), "v"(v1), "v"(v2), "v"(v3) : "memory");
  else if constexpr (POL == 2) asm volatile(DMA4(" sc1") :: "s"(lds), "s"(sbase), "v"(v0), "v"(v1), "v"(v2), "v"(v3) : "memory");
  else asm volatile(DMA4("") :: "s"(lds), "s"(sbase), "v"(v0), "v"(v1), "v"(v2), "v"(v3) : "memory");
}
DI void dma3(unsigned v0, unsigned v1, unsigned v2, const void* sbase, unsigned lds) {
  lds = __builtin_amdgcn_readfirstlane(lds);
  asm volatile(DMA_PRE "global_load_lds_dwordx4 %2, %1\n\tglobal_load_lds_dwordx4 %3, %1 offset:1024\n\tglobal_load_lds_dwordx4 %4, %1 offset:2048"
               :: "s"(lds), "s"(sbase), "v"(v0), "v"(v1), "v"(v2) : "memory");
}
DI void dma2nt(unsigned v0, const void* sbase, unsigned lds) {
  lds = __builtin_amdgcn_readfirstlane(lds);
  asm volatile(DMA_PRE "global_load_lds_dwordx4 %2, %1 nt\n\tglobal_load_lds_dwordx4 %2, %1 offset:1024 nt" :: "s"(lds), "s"(sbase), "v"(v0) : "memory");
}
template <int N> DI void waitvm() { asm volatile("s_waitcnt vmcnt(%0)" ::"i"(N) : "memory"); }
DI void bar() { asm volatile("s_waitcnt lgkmcnt(0)\n\ts_barrier" ::: "memory"); }
DI void waitlds() { asm volatile("s_waitcnt lgkmcnt(0)" ::: "memory"); }
DI unsigned pk2(float a, float b) { f32x2 t = {a, b}; bf16x2 p = __builtin_convertvector(t, bf16x2); return __builtin_bit_cast(unsigned, p); }
DI bf16x8 lds128(unsigned a) { return *(const LAS bf16x8*)(size_t)a; }
DI void sts128(unsigned a, u32x4 v) { *(LAS u32x4*)(size_t)a = v; }
DI bf16x8 ldstr(unsigned a0, unsigned a1) {
  bf16x4 a = __builtin_amdgcn_ds_read_tr16_b64_v4bf16((LAS bf16x4*)(size_t)a0);
  bf16x4 b = __builtin_amdgcn_ds_read_tr16_b64_v4bf16((LAS bf16x4*)(size_t)a1);
  return __builtin_shufflevector(a, b, 0, 1, 2, 3, 4, 5, 6, 7);
}
DI float xhalf_max(float v) { auto r = __builtin_amdgcn_permlane32_swap(__float_as_uint(v), __float_as_uint(v), false, false); return fmaxf(__uint_as_float(r[0]), __uint_as_float(r[1])); }
DI float xhalf_sum(float v) { auto r = __builtin_amdgcn_permlane32_swap(__float_as_uint(v), __float_as_uint(v), false, false); return __uint_as_float(r[0]) + __uint_as_float(r[1]); }
DI u32x4 pack8(const f32x16& a, int s) {
  u32x4 o = {pk2(a[8 * s], a[8 * s + 1]), pk2(a[8 * s + 2], a[8 * s + 3]), pk2(a[8 * s + 4], a[8 * s + 5]), pk2(a[8 * s + 6], a[8 * s + 7])};
  return o;
}
DI void pin(u32x4& x) { asm volatile("" : "+v"(x)); }
template <int NM, int ND> DI void ilv() {
#pragma unroll
  for (int i = 0; i < NM; ++i) { __builtin_amdgcn_sched_group_barrier(0x008, 1, 0); __builtin_amdgcn_sched_group_barrier(0x100, ND, 0); }
  __builtin_amdgcn_sched_barrier(0);
}
DI void st_sc1(__amdgpu_buffer_rsrc_t rs, unsigned off, u32x4 v) { __builtin_amdgcn_raw_buffer_store_b128(v, rs, off, 0, 16); }
DI u32x4 ld_sc1(__amdgpu_buffer_rsrc_t rs, unsigned off) { return __builtin_amdgcn_raw_buffer_load_b128(rs, off, 0, 16); }
#define RLX_AGENT __ATOMIC_RELAXED, __HIP_MEMORY_SCOPE_AGENT
constexpr unsigned SPIN_CAP = 1u << 18;

constexpr unsigned S(int i) { return 8 * KB * i; }
constexpr unsigned area(int a) { return a == 0 ? S(12) : a == 1 ? S(16) : a == 2 ? S(8) : a == 3 ? S(0) : S(4); }
constexpr unsigned AREA_TAB = 0x10243u;
constexpr unsigned QIMG = S(8), PB = S(8), PA = S(17), MISC = S(11);
constexpr unsigned L_MX = MISC, L_SM = MISC + 3072, L_TAG = MISC + 6144, L_CNT = MISC + 6400;
constexpr unsigned vslot(int j) { return j < 5 ? S(12 + j) : j < 13 ? S(j - 5) : S(12 + j - 13); }
constexpr unsigned oimg(int jj) { return jj == 0 ? S(12) : jj == 1 ? S(8) : jj == 2 ? S(0) : S(4); }
constexpr unsigned OIMG_TAB = 0x408Cu;
constexpr unsigned wobs(int jj) { return jj == 0 ? S(15) : S(16 + jj); }
constexpr unsigned wset(int set, int t) { return set == 0 ? (t == 0 ? S(16) : S(t - 1)) : S(3 + t); }
enum { C_O0 = 0, C_FREEX = 4, C_FREEY = 5, C_WOB = 6, C_REL0 = 7, C_L01 = 11, C_P01 = 12, C_L2 = 13, C_L3 = 14 };
DI void cnt_add(unsigned idx, unsigned n = 1) {
  unsigned long long keep; const unsigned a = L_CNT + idx * 4;
  asm volatile("s_mov_b64 %0, exec\n\ts_mov_b64 exec, 1\n\tds_add_u32 %1, %2\n\ts_mov_b64 exec, %0" : "=&s"(keep) : "v"(a), "v"(n) : "memory");
}
DI void cnt_wait(unsigned idx) {
  const unsigned a = L_CNT + idx * 4; unsigned v, s, n = 1u << 24;
  asm volatile("1:\n\tds_read_b32 %0, %3\n\ts_waitcnt lgkmcnt(0)\n\tv_readfirstlane_b32 %1, %0\n\ts_cmp_ge_u32 %1, 4\n\ts_cbranch_scc1 2f\n\t"
               "s_sleep 1\n\ts_sub_u32 %2, %2, 1\n\ts_cmp_lg_u32 %2, 0\n\ts_cbranch_scc1 1b\n2:"
               : "=&v"(v), "=&s"(s), "+s"(n) : "v"(a) : "memory", "scc");
}

__global__ __launch_bounds__(512, 2) void k_fused(const char* __restrict__ x, const char* __restrict__ kc, const char* __restrict__ vc,
                                                   const char* __restrict__ wq, const char* __restrict__ wo, char* __restrict__ ws, char* __restrict__ out) {
  const int g = blockIdx.x & 7, idx = blockIdx.x >> 3, b = idx >> 2, i4 = idx & 3, h = 4 * g + i4;
  const int c4 = (i4 + (b >> 1)) & 3;
  const bool tA = (i4 & 1) == 0;
  const int tid = threadIdx.x, lane = tid & 63;
  const int w = __builtin_amdgcn_readfirstlane(tid >> 6), dt = w & 3;
  const bool r1 = w >= 4;
  const int r = lane & 31, hh = lane >> 5;
  const int blk = (lane >> 4) & 1, q4 = (lane & 15) >> 2, p4 = lane & 3;
  const unsigned u = lane >> 4, l16 = lane * 16;
  STAMP(0);
  const char* kb = kc + ((size_t)(b * NH + h) * SK) * 256;
  const char* vb = vc + ((size_t)(b * NH + h) * SK) * 256;
  const unsigned vo_tr = u * 256 + (((lane & 15) ^ (u << 2)) * 16);
  const unsigned vqv = vo_tr + dt * 8192, vtv = vo_tr + dt * 2048;
  const unsigned k0v = u * 256 + (((lane & 15) ^ u) * 16) + dt * 8192, k1v = k0v ^ 64, k2v = k0v ^ 128, k3v = k0v ^ 192;
  auto karea = [&](int q) AI { return (q < 2 && tA) ? area(q) : area(q + 1); };
  auto issue_kq = [&](int q) AI {
    const unsigned d = karea(q) + dt * 8 * KB;
    dma4<1>(k0v, k1v, k2v, k3v, kb + q * 32768, d);
    dma4<1>(k0v, k1v, k2v, k3v, kb + q * 32768 + 4096, d + 4096);
  };
  auto issue_v = [&](int j) AI { dma2nt(vtv, vb + j * 8192, vslot(j) + dt * 2 * KB); };
  auto issue_vh = [&](int j0, int half) AI { dma4<1>(vqv, vqv, vqv, vqv, vb + j0 * 8192 + half * 4096, vslot(j0) + dt * 8 * KB + half * 4096); };
  bf16x8 kf[2][8];
  const unsigned kfo = dt * 8 * KB + r * 256 + ((hh ^ (r & 15)) * 16);
  auto pull_k = [&](int kt) AI {
    const unsigned slot = (r1 ? area(3 + kt) : karea(kt)) + kfo;
#pragma unroll
    for (int s = 0; s < 8; ++s) kf[kt][s] = lds128(slot ^ (s * 32));
  };

  if (!r1) {
    const unsigned epoch = __hip_atomic_load((GAS unsigned*)(ws + WS_EPOCH + (b * 8 + g) * 64), RLX_AGENT);
    if (!tA) { bar(); pull_k(0); bar(); pull_k(1); }
    STAMP(1);
    f32x16 qacc[3] = {};
    const int p4s = ((p4 & 1) << 1) | (p4 >> 1);
    const unsigned a_off = (8 * hh + q4) * 256 + (((4 * dt + 2 * blk + (p4s >> 1)) ^ (q4 << 2)) * 16) + 8 * (p4s & 1);
    const unsigned x_off = r * 128 + 16 * KB + ((hh ^ ((r >> 1) & 7)) * 16);
    bf16x8 fa[4] = {}, fb[4][3] = {};
    auto read_q = [&](unsigned base, int ks) AI {
      fa[ks] = ldstr(base + a_off + ks * 4096, base + a_off + ks * 4096 + 1024);
#pragma unroll
      for (int j = 0; j < 3; ++j) fb[ks][j] = lds128(base + (x_off ^ (ks * 32)) + j * 4096);
    };
    auto mma_q = [&](int ks) AI {
#pragma unroll
      for (int j = 0; j < 3; ++j) qacc[j] = MFMA(fa[ks], fb[ks][j], qacc[j]);
    };
    NOUNROLL
    for (int s0 = 0; s0 < 10; ++s0) {
      const unsigned base = ((AREA_TAB >> (4 * (s0 >= 5 ? s0 - 5 : s0))) & 15u) << 15;
      bar(); SB();
      read_q(base, 0); read_q(base, 1); mma_q(2); ilv<3, 4>();
      read_q(base, 2); mma_q(3); ilv<3, 2>();
      read_q(base, 3); mma_q(0); ilv<3, 2>();
      mma_q(1); SB();
    }
    mma_q(2); mma_q(3);
#pragma unroll
    for (int j = 0; j < 3; ++j)
#pragma unroll
      for (int sx = 0; sx < 2; ++sx) sts128(QIMG + (j * 8 + sx) * KB + dt * 2 * KB + l16, pack8(qacc[j], sx));
    if (tid < 16) *(LAS unsigned*)(size_t)(L_CNT + tid * 4) = 0u;
    if (tid == 0) *(LAS unsigned*)(size_t)L_TAG = epoch * 1664525u + 1013904223u;
    bar();
    STAMP(2);
  } else {
    const char* xb = x + (size_t)b * SQ * DM * 2;
    const char* wqh = wq + (size_t)h * 256;
    const unsigned vo_x = (lane >> 3) * 1280 + (((lane & 7) ^ u) * 16);
    const unsigned wq0 = u * 8192 + (((lane & 15) ^ (u << 2)) * 16) + dt * 16 * 8192, wq1 = wq0 + (32768 - 1024), wq2 = wq0 + 2 * (32768 - 1024), wq3 = wq0 + 3 * (32768 - 1024);
    const unsigned xb0 = (vo_x ^ ((dt & 1) << 6)) + dt * 3 * 10240, xb1 = (vo_x ^ (((dt + 1) & 1) << 6)) + dt * 3 * 10240 + (10240 - 1024), xb2 = xb0 + 2 * (10240 - 1024);
    const int rot = (b + 3 * i4 + g) % 10;
    auto issue_q = [&](int s0) AI {
      int s = s0 + rot; if (s >= 10) s -= 10;
      const unsigned base = ((AREA_TAB >> (4 * (s0 >= 5 ? s0 - 5 : s0))) & 15u) << 15;
      dma4<0>(wq0, wq1, wq2, wq3, wqh + (size_t)s * (64 * 8192), base + 4 * dt * KB);
      dma3(xb0, xb1, xb2, xb + s * 128, base + 16 * KB + 3 * dt * KB);
    };
    if (tA) {
      issue_q(0); issue_q(1); issue_q(2); waitvm<14>(); bar();
    } else {
      issue_kq(0); issue_kq(1); issue_kq(2);
      waitvm<16>(); bar(); issue_kq(3);
      waitvm<16>(); bar(); issue_q(0);
      waitvm<15>(); pull_k(0); issue_q(1);
      waitvm<14>(); pull_k(1);
      STAMP(1);
      waitvm<7>(); bar(); issue_q(2);
    }
    NOUNROLL
    for (int s0 = 0; s0 < 7; ++s0) { issue_q(s0 + 3); waitvm<14>(); bar(); }
    if (tA) {
      issue_kq(0); waitvm<15>(); bar();
      issue_kq(1); waitvm<16>(); bar();
      issue_kq(2); waitvm<16>(); bar();
      issue_kq(3); waitvm<16>(); bar();
    } else {
      waitvm<7>(); bar();
      issue_vh(0, 0); issue_vh(0, 1); waitvm<8>(); bar();
      issue_v(4); bar();
      issue_vh(5, 0); issue_vh(5, 1);
    }
    STAMP(2);
    bar();
  }

  f32x16 sacc[2][3] = {};
  {
    bf16x8 qf[2][6];
    auto rd_q6 = [&](int st) AI {
#pragma unroll
      for (int i = 0; i < 6; ++i) qf[st & 1][i] = lds128(QIMG + ((i % 3) * 8 + 2 * st + i / 3) * KB + l16);
    };
    auto s_tile = [&](int kt) AI {
      rd_q6(0); SB();
#pragma unroll
      for (int st = 0; st < 4; ++st) {
        if (st < 3) rd_q6(st + 1);
#pragma unroll
        for (int i = 0; i < 6; ++i) sacc[kt][i % 3] = MFMA(kf[kt][2 * st + i / 3], qf[st & 1][i], sacc[kt][i % 3]);
        ilv<6, 1>();
      }
    };
    if (tA) {
      if (r1) waitvm<8>(); pull_k(0); if (r1) issue_vh(0, 0);
      s_tile(0);
      if (r1) { issue_vh(0, 1); waitvm<8>(); } else bar(); pull_k(1); if (r1) issue_v(4);
      s_tile(1);
    } else {
      rd_q6(0); SB();
#pragma unroll
      for (int st = 0; st < 4; ++st) {
        if (st < 3) rd_q6(st + 1);
#pragma unroll
        for (int i = 0; i < 6; ++i) {
          sacc[0][i % 3] = MFMA(kf[0][2 * st + i / 3], qf[st & 1][i], sacc[0][i % 3]);
          sacc[1][i % 3] = MFMA(kf[1][2 * st + i / 3], qf[st & 1][i], sacc[1][i % 3]);
        }
        ilv<6, 1>();
      }
    }
  }
  const unsigned mxo = r * 32;
#pragma unroll
  for (int qb = 0; qb < 3; ++qb) {
    float m = sacc[0][qb][0];
#pragma unroll
    for (int kt = 0; kt < 2; ++kt)
#pragma unroll
      for (int i = 0; i < 16; ++i) m = fmaxf(m, sacc[kt][qb][i]);
    m = xhalf_max(m);
    if (hh == 0) *(LAS float*)(size_t)(L_MX + qb * 1024 + mxo + w * 4) = m;
  }
  if (r1) {
    if (tA) { issue_vh(5, 0); issue_vh(5, 1); waitvm<10>(); } else { issue_vh(9, 0); issue_vh(9, 1); waitvm<18>(); }
  }
  bar();
  STAMP(3);
  const float cs = 0.08838834764831845f * 1.4426950408889634f;
  auto max4 = [&](int qb, int half) AI {
    f32x4 m = *(const LAS f32x4*)(size_t)(L_MX + qb * 1024 + mxo + half * 16);
    return fmaxf(fmaxf(m[0], m[1]), fmaxf(m[2], m[3]));
  };
  float m0[3], lsum[3] = {0.f, 0.f, 0.f};
#pragma unroll
  for (int qb = 0; qb < 3; ++qb) m0[qb] = fmaxf(max4(qb, 0), max4(qb, r1));
  u32x4 pr[3][2];
  auto exp_tile = [&](int kt) AI {
#pragma unroll
    for (int qb = 0; qb < 3; ++qb) {
      const float nm = -m0[qb] * cs;
#pragma unroll
      for (int i = 0; i < 18; ++i) {
        if (i < 16) sacc[kt][qb][i] = __builtin_amdgcn_exp2f(fmaf(sacc[kt][qb][i], cs, nm));
        if (i >= 2) asm volatile("v_add_f32 %0, %0, %1" : "+v"(lsum[qb]) : "v"(sacc[kt][qb][i - 2]));
        if (i >= 2 && (i & 1)) pr[qb][(i - 2) >> 3][((i - 2) >> 1) & 3] = pk2(sacc[kt][qb][i - 3], sacc[kt][qb][i - 2]);
        SB();
      }
      pin(pr[qb][0]); pin(pr[qb][1]);
    }
  };
  auto put_p = [&](int kt) AI {
#pragma unroll
    for (int qb = 0; qb < 3; ++qb)
#pragma unroll
      for (int sx = 0; sx < 2; ++sx) sts128((kt == 0 ? PA : PB) + dt * 6 * KB + l16 + (sx * 3 + qb) * KB, pr[qb][sx]);
  };
  const char* wob = wo + (size_t)(4 * g) * 128 * 1280 + c4 * 320;
  const unsigned wa0 = u * 1280 + (((lane & 15) ^ (u << 2)) * 16) + dt * 32 * 1280, wa1 = wa0 + (5120 - 1024), wa2 = wa0 + 2 * (5120 - 1024), wa3 = wa0 + 3 * (5120 - 1024);
  auto issue_wa = [&](int jj) AI {
    const char* src = wob + (size_t)(((i4 + jj) & 3) * 128) * 1280;
    const unsigned d = (jj & 1) ? S(3) + dt * 8 * KB : (dt == 0 ? S(16) : (dt - 1) * 8 * KB);
    dma4<0>(wa0, wa1, wa2, wa3, src, d);
    dma4<0>(wa0, wa1, wa2, wa3, src + 16 * 1280, d + 4096);
  };
  if (r1 && tA) issue_vh(9, 0);
  exp_tile(0);
  if (r1) {
    if (tA) issue_vh(9, 1);
    cnt_wait(C_REL0); issue_v(13); issue_v(14); issue_v(15);
    waitvm<14>();
    bar();
  }
  put_p(0);
  exp_tile(1);
#pragma unroll
  for (int qb = 0; qb < 3; ++qb) {
    const float s = xhalf_sum(lsum[qb]);
    if (hh == 0) *(LAS float*)(size_t)(L_SM + qb * 1024 + mxo + w * 4) = s;
  }
  if (r1) { cnt_wait(C_REL0 + 1); issue_wa(0); waitvm<14>(); }
  STAMP(4);
  bar();
  STAMP(5);
  put_p(1);

  const unsigned v_off = (4 * hh + q4) * 256 + (((4 * dt + 2 * blk + (p4 >> 1)) ^ (q4 << 2)) * 16) + 8 * (p4 & 1);
  const unsigned tag = __builtin_amdgcn_readfirstlane(*(const LAS unsigned*)(size_t)L_TAG);
  f32x16 acc[3] = {};
  if (!r1) {
    f32x16 oa[2][3] = {};
    bf16x8 vf[4][2], pf[2][2][3];
    auto rd_v = [&](int j) AI {
#pragma unroll
      for (int sx = 0; sx < 2; ++sx) vf[j & 3][sx] = ldstr(vslot(j) + v_off + sx * 4096, vslot(j) + v_off + sx * 4096 + 2048);
    };
    auto rd_p = [&](int j) AI {
#pragma unroll
      for (int sx = 0; sx < 2; ++sx)
#pragma unroll
        for (int qb = 0; qb < 3; ++qb) pf[j & 1][sx][qb] = lds128((((j >> 2) & 1) ? PB : PA) + ((j & 3) * 6 + sx * 3 + qb) * KB + l16);
    };
    auto mma_pv = [&](int j) AI {
#pragma unroll
      for (int sx = 0; sx < 2; ++sx)
#pragma unroll
        for (int qb = 0; qb < 3; ++qb) oa[j >> 3][qb] = MFMA(vf[j & 3][sx], pf[j & 1][sx][qb], oa[j >> 3][qb]);
    };
    auto pv_quarter = [&](int q) AI {
      rd_v(4 * q); rd_p(4 * q); rd_v(4 * q + 1); rd_v(4 * q + 2);
      if (q > 0) { mma_pv(4 * q - 1); ilv<6, 3>(); } else SB();
      rd_v(4 * q + 3); rd_p(4 * q + 1); mma_pv(4 * q); ilv<6, 2>();
      rd_p(4 * q + 2); mma_pv(4 * q + 1); ilv<6, 1>();
      asm volatile("s_waitcnt lgkmcnt(6)" ::: "memory"); cnt_add(C_REL0 + q); SB();
      rd_p(4 * q + 3); mma_pv(4 * q + 2); ilv<6, 1>();
    };
    pv_quarter(0); bar(); SB();
    pv_quarter(1); bar(); SB();
    STAMP(6);
    pv_quarter(2);
    float sc0[3], sc1[3];
#pragma unroll
    for (int qb = 0; qb < 3; ++qb) {
      const float m = fmaxf(m0[qb], max4(qb, 1));
      const float al = __builtin_amdgcn_exp2f((m0[qb] - m) * cs);
      f32x4 l0 = *(const LAS f32x4*)(size_t)(L_SM + qb * 1024 + mxo);
      f32x4 l1 = *(const LAS f32x4*)(size_t)(L_SM + qb * 1024 + mxo + 16);
      const float l = al * ((l0[0] + l0[1]) + (l0[2] + l0[3])) + ((l1[0] + l1[1]) + (l1[2] + l1[3]));
      sc1[qb] = 1.0f / l; sc0[qb] = al * sc1[qb];
    }
    bar(); SB();
    STAMP(7);
    pv_quarter(3);
#pragma unroll
    for (int qb = 0; qb < 3; ++qb)
#pragma unroll
      for (int i = 0; i < 16; ++i) oa[0][qb][i] *= sc0[qb];
    mma_pv(15);
    const char* obase = ws + WS_O + (size_t)(b * NH + 4 * g) * 24576;
    const __amdgpu_buffer_rsrc_t ors = __builtin_amdgcn_make_buffer_rsrc((char*)obase, 0, 4 * 24576, 0x00020000);
    cnt_wait(C_REL0 + 3);
#pragma unroll
    for (int qb = 0; qb < 3; ++qb) {
#pragma unroll
      for (int i = 0; i < 16; ++i) oa[1][qb][i] = fmaf(oa[1][qb][i], sc1[qb], oa[0][qb][i]);
#pragma unroll
      for (int sx = 0; sx < 2; ++sx) {
        const u32x4 o = pack8(oa[1][qb], sx);
        st_sc1(ors, i4 * 24576 + (qb * 8 + sx) * KB + dt * 2 * KB + l16, o);
        sts128(oimg(0) + (qb * 8 + sx) * KB + dt * 2 * KB + l16, o);
      }
    }
    waitlds(); cnt_add(C_O0);
    STAMP(8);
    waitvm<0>();
    GAS unsigned* fl = (GAS unsigned*)(ws + WS_F1 + (size_t)(b * 8 + g) * 64);
    if (lane == 0) __hip_atomic_store(fl + dt * 4 + i4, tag, RLX_AGENT);
    STAMP(9);
    if (w == 3) {
      unsigned pend = 0xEu;
      cnt_wait(C_O0);
      NOUNROLL
      for (unsigned spin = 0; pend && spin < SPIN_CAP; ++spin) {
        const unsigned fv = __hip_atomic_load(fl + (lane & 12) + ((i4 + lane) & 3), RLX_AGENT);
        const unsigned m = (unsigned)__ballot(fv == tag);
        const unsigned rdy = m & (m >> 4) & (m >> 8) & (m >> 12) & pend;
        if (!rdy) { __builtin_amdgcn_s_sleep(1); continue; }
        NOUNROLL
        for (unsigned t = rdy; t; t &= t - 1) {
          const unsigned jj = __builtin_ctz(t);
          if (jj >= 2) cnt_wait(C_FREEX + jj - 2);
          const char* src = obase + ((i4 + jj) & 3) * 24576;
          const unsigned d = ((OIMG_TAB >> (4 * jj)) & 15u) << 13;
          NOUNROLL
          for (int i = 0; i < 6; ++i) dma4<2>(l16, l16, l16, l16, src + i * 4096, d + i * 4096);
        }
        waitvm<0>();
        NOUNROLL
        for (unsigned t = rdy; t; t &= t - 1) cnt_add(__builtin_ctz(t), 4);
        pend &= ~rdy;
      }
      if (i4 == 0 && lane == 0) __hip_atomic_store((GAS unsigned*)(ws + WS_EPOCH + (b * 8 + g) * 64), tag, RLX_AGENT);
    } else {
      const unsigned w_off = (4 * hh + q4) * 64 + (2 * blk + (p4 >> 1)) * 16 + 8 * (p4 & 1);
      cnt_wait(C_WOB);
      NOUNROLL
      for (int jj = 0; jj < 4; ++jj) {
        cnt_wait(jj);
        const unsigned wb = (jj == 0 ? S(15) : S(16) + jj * 8 * KB) + w_off;
        const unsigned ob = (((OIMG_TAB >> (4 * jj)) & 15u) << 13) + dt * 8 * KB + l16;
        bf16x8 af[8], of[8];
#pragma unroll
        for (int ds = 0; ds < 8; ++ds) { af[ds] = ldstr(wb + ds * KB, wb + ds * KB + 512); of[ds] = lds128(ob + ds * KB); }
#pragma unroll
        for (int ds = 0; ds < 8; ++ds) acc[0] = MFMA(af[ds], of[ds], acc[0]);
      }
    }
  } else {
    bf16x8 wf[4][8];
    auto pull_wa = [&](int jj) AI {
#pragma unroll
      for (int ds = 0; ds < 8; ++ds) { const unsigned a = wset(jj & 1, ds >> 1) + v_off + (ds & 1) * 4096; wf[jj][ds] = ldstr(a, a + 2048); }
    };
    auto mma_o = [&](int jj) AI {
      bf16x8 of[3][3];
#pragma unroll
      for (int ds = 0; ds < 2; ++ds)
#pragma unroll
        for (int qb = 0; qb < 3; ++qb) of[ds][qb] = lds128(oimg(jj) + (qb * 8 + ds) * KB + l16);
      SB();
#pragma unroll
      for (int ds = 0; ds < 8; ++ds) {
        if (ds < 6) {
#pragma unroll
          for (int qb = 0; qb < 3; ++qb) of[(ds + 2) % 3][qb] = lds128(oimg(jj) + (qb * 8 + ds + 2) * KB + l16);
        }
#pragma unroll
        for (int qb = 0; qb < 3; ++qb) acc[qb] = MFMA(wf[jj][ds], of[ds % 3][qb], acc[qb]);
        ilv<3, 1>();
      }
    };
    cnt_wait(C_REL0 + 2); issue_wa(1);
    waitvm<16>();
    bar();
    STAMP(6);
    {
      const unsigned wb0 = (lane >> 2) * 1280 + (lane & 3) * 16 + 256, wb1 = wb0 + (20480 - 1024), wb2 = wb0 + 2 * (20480 - 1024), wb3 = wb0 + 3 * (20480 - 1024);
      const char* src = wob + (size_t)(((i4 + dt) & 3) * 128) * 1280;
      const unsigned d = dt == 0 ? S(15) : S(16) + dt * 8 * KB;
      dma4<0>(wb0, wb1, wb2, wb3, src, d);
      dma4<0>(wb0, wb1, wb2, wb3, src + 64 * 1280, d + 4096);
    }
    waitvm<8>(); cnt_add(C_L01); cnt_wait(C_L01);
    pull_wa(0); pull_wa(1); waitlds(); cnt_add(C_P01); cnt_wait(C_P01);
    STAMP(7);
    issue_wa(2); issue_wa(3);
    waitvm<16>(); cnt_add(C_WOB);
    waitvm<8>(); cnt_add(C_L2); cnt_wait(C_L2); pull_wa(2); waitlds(); cnt_add(C_FREEX);
    waitvm<0>(); cnt_add(C_L3); cnt_wait(C_L3); pull_wa(3); waitlds(); cnt_add(C_FREEY);
    STAMP(8);
    cnt_wait(0); mma_o(0);
    STAMP(9);
    cnt_wait(1); mma_o(1);
    STAMP(10);
    cnt_wait(2); mma_o(2);
    STAMP(11);
    cnt_wait(3); mma_o(3);
  }
  STAMP(12);
  if (w != 3) {
    const __amdgpu_buffer_rsrc_t prs = __builtin_amdgcn_make_buffer_rsrc(ws + WS_P + (size_t)(b * 4 + c4) * PBYTES, 0, PBYTES, 0x00020000);
    const unsigned so = (((r >> 2) * 8 + w) * 6) * KB + (g * 8 + (r & 3) + 4 * hh) * 16;
#pragma unroll
    for (int t = 0; t < 3; ++t) if (t == 0 || r1) {
#pragma unroll
      for (int uu = 0; uu < 2; ++uu) st_sc1(prs, so + (t * 2 + uu) * KB, pack8(acc[t], uu));
    }
  }
}

__global__ __launch_bounds__(512) void k_reduce(const char* __restrict__ ws, char* __restrict__ out) {
  const int g = blockIdx.x & 7, idx = blockIdx.x >> 3, b = idx >> 2, c4 = idx & 3;
  const int lane = threadIdx.x & 63, w = __builtin_amdgcn_readfirstlane(threadIdx.x >> 6), dt = w & 3;
  const bool r1 = w >= 4;
  if (w == 3) return;
  const char* pb = ws + WS_P + (size_t)(b * 4 + c4) * PBYTES + ((g * 8 + w) * 6) * KB + lane * 16;
  u32x4 t[3][2];
#pragma unroll
  for (int tt = 0; tt < 3; ++tt)
#pragma unroll
    for (int uu = 0; uu < 2; ++uu) t[tt][uu] = *(const u32x4*)(pb + ((r1 ? tt : 0) * 2 + uu) * KB);
  char* orow = out + ((size_t)(b * SQ + (r1 ? 0 : 32 * dt) + 4 * g + (lane & 3)) * DM + 160 * c4 + (r1 ? 32 * dt : 128) + 8 * (lane >> 4) + (lane & 4)) * 2;
#pragma unroll
  for (int tt = 0; tt < 3; ++tt) if (tt == 0 || r1) {
    float xx[16], y[8], z[4];
#pragma unroll
    for (int i = 0; i < 8; ++i) { const unsigned d = t[tt][i >> 2][i & 3]; xx[2 * i] = __uint_as_float(d << 16); xx[2 * i + 1] = __uint_as_float(d & 0xffff0000u); }
#pragma unroll
    for (int i = 0; i < 8; ++i) { auto q = __builtin_amdgcn_permlane32_swap(__float_as_uint(xx[i]), __float_as_uint(xx[i + 8]), false, false); y[i] = __uint_as_float(q[0]) + __uint_as_float(q[1]); }
#pragma unroll
    for (int i = 0; i < 4; ++i) { auto q = __builtin_amdgcn_permlane16_swap(__float_as_uint(y[i]), __float_as_uint(y[i + 4]), false, false); z[i] = __uint_as_float(q[0]) + __uint_as_float(q[1]); }
#pragma unroll
    for (int i = 0; i < 4; ++i) z[i] += __uint_as_float(__builtin_amdgcn_update_dpp(0u, __float_as_uint(z[i]), 0x128, 0xf, 0xf, false));
    const u32x2 o = {pk2(z[0], z[1]), pk2(z[2], z[3])};
    if ((lane & 8) == 0) *(u32x2*)(orow + (size_t)tt * 32 * DM * 2) = o;
  }
}

extern "C" void kernel_launch(void* const* d_in, const int* in_sizes, int n_in, void* d_out, int out_size, void* d_ws, size_t ws_size,
                              hipStream_t stream) {
  (void)hipFuncSetAttribute((const void*)k_fused, hipFuncAttributeMaxDynamicSharedMemorySize, LDS_BYTES);
  hipLaunchKernelGGL(k_fused, dim3(256), dim3(512), LDS_BYTES, stream, (const char*)d_in[0], (const char*)d_in[1], (const char*)d_in[2],
                     (const char*)d_in[3], (const char*)d_in[4], (char*)d_ws, (char*)d_out);
  hipLaunchKernelGGL(k_reduce, dim3(256), dim3(512), 0, stream, (const char*)d_ws, (char*)d_out);
}
```

```cpp
#include <hip/hip_runtime.h>
#include <hip/hip_bf16.h>

typedef __attribute__((ext_vector_type(8))) __bf16 bf16x8;
typedef __attribute__((ext_vector_type(4))) __bf16 bf16x4;
typedef __attribute__((ext_vector_type(2))) __bf16 bf16x2;
typedef __attribute__((ext_vector_type(16))) float f32x16;
typedef __attribute__((ext_vector_type(4))) float f32x4;
typedef __attribute__((ext_vector_type(2))) float f32x2;
typedef __attribute__((ext_vector_type(4))) unsigned u32x4;
typedef __attribute__((ext_vector_type(2))) unsigned u32x2;
#define LAS __attribute__((address_space(3)))
#define GAS __attribute__((address_space(1)))
#define DI __device__ __forceinline__
#define AI __attribute__((always_inline))
#define MFMA(a, b, c) __builtin_amdgcn_mfma_f32_32x32x16_bf16((a), (b), (c), 0, 0, 0)
#define SB() __builtin_amdgcn_sched_barrier(0)
#define NOUNROLL _Pragma("clang loop unroll(disable)")

#define STAMP(i) do {} while (0)

constexpr int SQ = 96, DM = 640, NH = 32, SK = 512;
constexpr unsigned KB = 1024;
constexpr unsigned LDS_BYTES = 163840;

constexpr size_t WS_EPOCH = 0;
constexpr size_t WS_F1 = 4096;
constexpr size_t WS_F2 = 16384;
constexpr size_t WS_O = 1u << 20;
constexpr size_t WS_P = 8u << 20;
constexpr unsigned PBYTES = 8 * 8 * 3 * 2 * 1024;

#define DMA_PRE "s_mov_b32 m0, %0\n\ts_nop 0\n\t"
#define DMA4(SUF) DMA_PRE "global_load_lds_dwordx4 %2, %1" SUF "\n\tglobal_load_lds_dwordx4 %3, %1 offset:1024" SUF \
                  "\n\tglobal_load_lds_dwordx4 %4, %1 offset:2048" SUF "\n\tglobal_load_lds_dwordx4 %5, %1 offset:3072" SUF
template <int POL> DI void dma4(unsigned v0, unsigned v1, unsigned v2, unsigned v3, const void* sbase, unsigned lds) {
  lds = __builtin_amdgcn_readfirstlane(lds);
  if constexpr (POL == 1) asm volatile(DMA4(" nt") :: "s"(lds), "s"(sbase), "v"(v0), "v"(v1), "v"(v2), "v"(v3) : "memory");
  else if constexpr (POL == 2) asm volatile(DMA4(" sc1") :: "s"(lds), "s"(sbase), "v"(v0), "v"(v1), "v"(v2), "v"(v3) : "memory");
  else asm volatile(DMA4("") :: "s"(lds), "s"(sbase), "v"(v0), "v"(v1), "v"(v2), "v"(v3) : "memory");
}
DI void dma3(unsigned v0, unsigned v1, unsigned v2, const void* sbase, unsigned lds) {
  lds = __builtin_amdgcn_readfirstlane(lds);
  asm volatile(DMA_PRE "global_load_lds_dwordx4 %2, %1\n\tglobal_load_lds_dwordx4 %3, %1 offset:1024\n\tglobal_load_lds_dwordx4 %4, %1 offset:2048"
               :: "s"(lds), "s"(sbase), "v"(v0), "v"(v1), "v"(v2) : "memory");
}
DI void dma2nt(unsigned v0, const void* sbase, unsigned lds) {
  lds = __builtin_amdgcn_readfirstlane(lds);
  asm volatile(DMA_PRE "global_load_lds_dwordx4 %2, %1 nt\n\tglobal_load_lds_dwordx4 %2, %1 offset:1024 nt" :: "s"(lds), "s"(sbase), "v"(v0) : "memory");
}
template <int N> DI void waitvm() { asm volatile("s_waitcnt vmcnt(%0)" ::"i"(N) : "memory"); }
DI void bar() { asm volatile("s_waitcnt lgkmcnt(0)\n\ts_barrier" ::: "memory"); }
DI void waitlds() { asm volatile("s_waitcnt lgkmcnt(0)" ::: "memory"); }
DI unsigned pk2(float a, float b) { f32x2 t = {a, b}; bf16x2 p = __builtin_convertvector(t, bf16x2); return __builtin_bit_cast(unsigned, p); }
DI bf16x8 lds128(unsigned a) { return *(const LAS bf16x8*)(size_t)a; }
DI void sts128(unsigned a, u32x4 v) { *(LAS u32x4*)(size_t)a = v; }
DI bf16x8 ldstr(unsigned a0, unsigned a1) {
  bf16x4 a = __builtin_amdgcn_ds_read_tr16_b64_v4bf16((LAS bf16x4*)(size_t)a0);
  bf16x4 b = __builtin_amdgcn_ds_read_tr16_b64_v4bf16((LAS bf16x4*)(size_t)a1);
  return __builtin_shufflevector(a, b, 0, 1, 2, 3, 4, 5, 6, 7);
}
DI float xhalf_max(float v) { auto r = __builtin_amdgcn_permlane32_swap(__float_as_uint(v), __float_as_uint(v), false, false); return fmaxf(__uint_as_float(r[0]), __uint_as_float(r[1])); }
DI float xhalf_sum(float v) { auto r = __builtin_amdgcn_permlane32_swap(__float_as_uint(v), __float_as_uint(v), false, false); return __uint_as_float(r[0]) + __uint_as_float(r[1]); }
DI u32x4 pack8(const f32x16& a, int s) {
  u32x4 o = {pk2(a[8 * s], a[8 * s + 1]), pk2(a[8 * s + 2], a[8 * s + 3]), pk2(a[8 * s + 4], a[8 * s + 5]), pk2(a[8 * s + 6], a[8 * s + 7])};
  return o;
}
DI void pin(u32x4& x) { asm volatile("" : "+v"(x)); }
template <int NM, int ND> DI void ilv() {
#pragma unroll
  for (int i = 0; i < NM; ++i) { __builtin_amdgcn_sched_group_barrier(0x008, 1, 0); __builtin_amdgcn_sched_group_barrier(0x100, ND, 0); }
  __builtin_amdgcn_sched_barrier(0);
}
DI void st_sc1(__amdgpu_buffer_rsrc_t rs, unsigned off, u32x4 v) { __builtin_amdgcn_raw_buffer_store_b128(v, rs, off, 0, 16); }
DI u32x4 ld_sc1(__amdgpu_buffer_rsrc_t rs, unsigned off) { return __builtin_amdgcn_raw_buffer_load_b128(rs, off, 0, 16); }
#define RLX_AGENT __ATOMIC_RELAXED, __HIP_MEMORY_SCOPE_AGENT
constexpr unsigned SPIN_CAP = 1u << 18;

constexpr unsigned S(int i) { return 8 * KB * i; }
constexpr unsigned area(int a) { return a == 0 ? S(12) : a == 1 ? S(16) : a == 2 ? S(8) : a == 3 ? S(0) : S(4); }
constexpr unsigned AREA_TAB = 0x10243u;
constexpr unsigned QIMG = S(8), PB = S(8), PA = S(17), MISC = S(11);
constexpr unsigned L_MX = MISC, L_SM = MISC + 3072, L_TAG = MISC + 6144, L_CNT = MISC + 6400;
constexpr unsigned vslot(int j) { return j < 5 ? S(12 + j) : j < 13 ? S(j - 5) : S(12 + j - 13); }
constexpr unsigned oimg(int jj) { return jj == 0 ? S(12) : jj == 1 ? S(8) : jj == 2 ? S(0) : S(4); }
constexpr unsigned OIMG_TAB = 0x408Cu;
constexpr unsigned wobs(int jj) { return jj == 0 ? S(15) : S(16 + jj); }
constexpr unsigned wset(int set, int t) { return set == 0 ? (t == 0 ? S(16) : S(t - 1)) : S(3 + t); }
enum { C_O0 = 0, C_FREEX = 4, C_FREEY = 5, C_WOB = 6, C_REL0 = 7, C_L01 = 11, C_P01 = 12, C_L2 = 13, C_L3 = 14 };
DI void cnt_add(unsigned idx, unsigned n = 1) {
  unsigned long long keep; const unsigned a = L_CNT + idx * 4;
  asm volatile("s_mov_b64 %0, exec\n\ts_mov_b64 exec, 1\n\tds_add_u32 %1, %2\n\ts_mov_b64 exec, %0" : "=&s"(keep) : "v"(a), "v"(n) : "memory");
}
DI void cnt_wait(unsigned idx) {
  const unsigned a = L_CNT + idx * 4; unsigned v, s, n = 1u << 24;
  asm volatile("1:\n\tds_read_b32 %0, %3\n\ts_waitcnt lgkmcnt(0)\n\tv_readfirstlane_b32 %1, %0\n\ts_cmp_ge_u32 %1, 4\n\ts_cbranch_scc1 2f\n\t"
               "s_sleep 1\n\ts_sub_u32 %2, %2, 1\n\ts_cmp_lg_u32 %2, 0\n\ts_cbranch_scc1 1b\n2:"
               : "=&v"(v), "=&s"(s), "+s"(n) : "v"(a) : "memory", "scc");
}

__global__ __launch_bounds__(512, 2) void k_fused(const char* __restrict__ x, const char* __restrict__ kc, const char* __restrict__ vc,
                                                   const char* __restrict__ wq, const char* __restrict__ wo, char* __restrict__ ws, char* __restrict__ out) {
  const int g = blockIdx.x & 7, idx = blockIdx.x >> 3, b = idx >> 2, i4 = idx & 3, h = 4 * g + i4;
  const int c4 = (i4 + (b >> 1)) & 3;
  const bool tA = (i4 & 1) == 0;
  const int tid = threadIdx.x, lane = tid & 63;
  const int w = __builtin_amdgcn_readfirstlane(tid >> 6), dt = w & 3;
  const bool r1 = w >= 4;
  const int r = lane & 31, hh = lane >> 5;
  const int blk = (lane >> 4) & 1, q4 = (lane & 15) >> 2, p4 = lane & 3;
  const unsigned u = lane >> 4, l16 = lane * 16;
  STAMP(0);
  const char* kb = kc + ((size_t)(b * NH + h) * SK) * 256;
  const char* vb = vc + ((size_t)(b * NH + h) * SK) * 256;
  const unsigned vo_tr = u * 256 + (((lane & 15) ^ (u << 2)) * 16);
  const unsigned vqv = vo_tr + dt * 8192, vtv = vo_tr + dt * 2048;
  const unsigned k0v = u * 256 + (((lane & 15) ^ u) * 16) + dt * 8192, k1v = k0v ^ 64, k2v = k0v ^ 128, k3v = k0v ^ 192;
  auto karea = [&](int q) AI { return (q < 2 && tA) ? area(q) : area(q + 1); };
  auto issue_kq = [&](int q) AI {
    const unsigned d = karea(q) + dt * 8 * KB;
    dma4<1>(k0v, k1v, k2v, k3v, kb + q * 32768, d);
    dma4<1>(k0v, k1v, k2v, k3v, kb + q * 32768 + 4096, d + 4096);
  };
  auto issue_v = [&](int j) AI { dma2nt(vtv, vb + j * 8192, vslot(j) + dt * 2 * KB); };
  auto issue_vh = [&](int j0, int half) AI { dma4<1>(vqv, vqv, vqv, vqv, vb + j0 * 8192 + half * 4096, vslot(j0) + dt * 8 * KB + half * 4096); };
  bf16x8 kf[2][8];
  const unsigned kfo = dt * 8 * KB + r * 256 + ((hh ^ (r & 15)) * 16);
  auto pull_k = [&](int kt) AI {
    const unsigned slot = (r1 ? area(3 + kt) : karea(kt)) + kfo;
#pragma unroll
    for (int s = 0; s < 8; ++s) kf[kt][s] = lds128(slot ^ (s * 32));
  };

  if (!r1) {
    const unsigned epoch = __hip_atomic_load((GAS unsigned*)(ws + WS_EPOCH + (b * 8 + g) * 64), RLX_AGENT);
    {
      unsigned long long pc; unsigned len;
      asm volatile("s_getpc_b64 %0\n.Lk1_here:\n\ts_mov_b32 %1, .Lk2_end-.Lk1_here" : "=s"(pc), "=s"(len));
      const unsigned off = (idx * 4 + dt) * 512 + (lane & 7) * 64;
      unsigned sink = 0;
      if (lane < 8 && off < len) sink = *(const GAS unsigned*)(pc + off);
      if (0 && lane >= 8 && lane < 8 + 12) sink = *(const GAS unsigned*)(ws + (size_t)(lane - 8) * (2u << 20) + (1u << 20) - 4096 + idx * 64);
      asm volatile("" :: "v"(sink));
    }
    if (!tA) { bar(); pull_k(0); bar(); pull_k(1); }
    STAMP(1);
    f32x16 qacc[3] = {};
    const int p4s = ((p4 & 1) << 1) | (p4 >> 1);
    const unsigned a_off = (8 * hh + q4) * 256 + (((4 * dt + 2 * blk + (p4s >> 1)) ^ (q4 << 2)) * 16) + 8 * (p4s & 1);
    const unsigned x_off = r * 128 + 16 * KB + ((hh ^ ((r >> 1) & 7)) * 16);
    bf16x8 fa[4] = {}, fb[4][3] = {};
    auto read_q = [&](unsigned base, int ks) AI {
      fa[ks] = ldstr(base + a_off + ks * 4096, base + a_off + ks * 4096 + 1024);
#pragma unroll
      for (int j = 0; j < 3; ++j) fb[ks][j] = lds128(base + (x_off ^ (ks * 32)) + j * 4096);
    };
    auto mma_q = [&](int ks) AI {
#pragma unroll
      for (int j = 0; j < 3; ++j) qacc[j] = MFMA(fa[ks], fb[ks][j], qacc[j]);
    };
    NOUNROLL
    for (int s0 = 0; s0 < 10; ++s0) {
      const unsigned base = ((AREA_TAB >> (4 * (s0 >= 5 ? s0 - 5 : s0))) & 15u) << 15;
      bar(); SB();
      read_q(base, 0); read_q(base, 1); mma_q(2); ilv<3, 4>();
      read_q(base, 2); mma_q(3); ilv<3, 2>();
      read_q(base, 3); mma_q(0); ilv<3, 2>();
      mma_q(1); SB();
    }
    mma_q(2); mma_q(3);
#pragma unroll
    for (int j = 0; j < 3; ++j)
#pragma unroll
      for (int sx = 0; sx < 2; ++sx) sts128(QIMG + (j * 8 + sx) * KB + dt * 2 * KB + l16, pack8(qacc[j], sx));
    if (tid < 16) *(LAS unsigned*)(size_t)(L_CNT + tid * 4) = 0u;
    if (tid == 0) *(LAS unsigned*)(size_t)L_TAG = epoch * 1664525u + 1013904223u;
    bar();
    STAMP(2);
  } else {
    const char* xb = x + (size_t)b * SQ * DM * 2;
    const char* wqh = wq + (size_t)h * 256;
    const unsigned vo_x = (lane >> 3) * 1280 + (((lane & 7) ^ u) * 16);
    const unsigned wq0 = u * 8192 + (((lane & 15) ^ (u << 2)) * 16) + dt * 16 * 8192, wq1 = wq0 + (32768 - 1024), wq2 = wq0 + 2 * (32768 - 1024), wq3 = wq0 + 3 * (32768 - 1024);
    const unsigned xb0 = (vo_x ^ ((dt & 1) << 6)) + dt * 3 * 10240, xb1 = (vo_x ^ (((dt + 1) & 1) << 6)) + dt * 3 * 10240 + (10240 - 1024), xb2 = xb0 + 2 * (10240 - 1024);
    const int rot = (b + 3 * i4 + g) % 10;
    auto issue_q = [&](int s0) AI {
      int s = s0 + rot; if (s >= 10) s -= 10;
      const unsigned base = ((AREA_TAB >> (4 * (s0 >= 5 ? s0 - 5 : s0))) & 15u) << 15;
      dma4<0>(wq0, wq1, wq2, wq3, wqh + (size_t)s * (64 * 8192), base + 4 * dt * KB);
      dma3(xb0, xb1, xb2, xb + s * 128, base + 16 * KB + 3 * dt * KB);
    };
    if (tA) {
      issue_q(0); issue_q(1); issue_q(2); waitvm<14>(); bar();
    } else {
      issue_kq(0); issue_kq(1); issue_kq(2);
      waitvm<16>(); bar(); issue_kq(3);
      waitvm<16>(); bar(); issue_q(0);
      waitvm<15>(); pull_k(0); issue_q(1);
      waitvm<14>(); pull_k(1);
      STAMP(1);
      waitvm<7>(); bar(); issue_q(2);
    }
    NOUNROLL
    for (int s0 = 0; s0 < 7; ++s0) { issue_q(s0 + 3); waitvm<14>(); bar(); }
    if (tA) {
      issue_kq(0); waitvm<15>(); bar();
      issue_kq(1); waitvm<16>(); bar();
      issue_kq(2); waitvm<16>(); bar();
      issue_kq(3); waitvm<16>(); bar();
    } else {
      waitvm<7>(); bar();
      issue_vh(0, 0); issue_vh(0, 1); waitvm<8>(); bar();
      issue_v(4); bar();
      issue_vh(5, 0); issue_vh(5, 1);
    }
    STAMP(2);
    bar();
  }

  f32x16 sacc[2][3] = {};
  {
    bf16x8 qf[2][6];
    auto rd_q6 = [&](int st) AI {
#pragma unroll
      for (int i = 0; i < 6; ++i) qf[st & 1][i] = lds128(QIMG + ((i % 3) * 8 + 2 * st + i / 3) * KB + l16);
    };
    auto s_tile = [&](int kt) AI {
      rd_q6(0); SB();
#pragma unroll
      for (int st = 0; st < 4; ++st) {
        if (st < 3) rd_q6(st + 1);
#pragma unroll
        for (int i = 0; i < 6; ++i) sacc[kt][i % 3] = MFMA(kf[kt][2 * st + i / 3], qf[st & 1][i], sacc[kt][i % 3]);
        ilv<6, 1>();
      }
    };
    if (tA) {
      if (r1) waitvm<8>(); pull_k(0); if (r1) issue_vh(0, 0);
      s_tile(0);
      if (r1) { issue_vh(0, 1); waitvm<8>(); } else bar(); pull_k(1); if (r1) issue_v(4);
      s_tile(1);
    } else {
      rd_q6(0); SB();
#pragma unroll
      for (int st = 0; st < 4; ++st) {
        if (st < 3) rd_q6(st + 1);
#pragma unroll
        for (int i = 0; i < 6; ++i) {
          sacc[0][i % 3] = MFMA(kf[0][2 * st + i / 3], qf[st & 1][i], sacc[0][i % 3]);
          sacc[1][i % 3] = MFMA(kf[1][2 * st + i / 3], qf[st & 1][i], sacc[1][i % 3]);
        }
        ilv<6, 1>();
      }
    }
  }
  const unsigned mxo = r * 32;
#pragma unroll
  for (int qb = 0; qb < 3; ++qb) {
    float m = sacc[0][qb][0];
#pragma unroll
    for (int kt = 0; kt < 2; ++kt)
#pragma unroll
      for (int i = 0; i < 16; ++i) m = fmaxf(m, sacc[kt][qb][i]);
    m = xhalf_max(m);
    if (hh == 0) *(LAS float*)(size_t)(L_MX + qb * 1024 + mxo + w * 4) = m;
  }
  if (r1) {
    if (tA) { issue_vh(5, 0); issue_vh(5, 1); waitvm<10>(); } else { issue_vh(9, 0); issue_vh(9, 1); waitvm<18>(); }
  }
  bar();
  STAMP(3);
  const float cs = 0.08838834764831845f * 1.4426950408889634f;
  auto max4 = [&](int qb, int half) AI {
    f32x4 m = *(const LAS f32x4*)(size_t)(L_MX + qb * 1024 + mxo + half * 16);
    return fmaxf(fmaxf(m[0], m[1]), fmaxf(m[2], m[3]));
  };
  float m0[3], lsum[3] = {0.f, 0.f, 0.f};
#pragma unroll
  for (int qb = 0; qb < 3; ++qb) m0[qb] = fmaxf(max4(qb, 0), max4(qb, r1));
  u32x4 pr[3][2];
  auto exp_tile = [&](int kt) AI {
#pragma unroll
    for (int qb = 0; qb < 3; ++qb) {
      const float nm = -m0[qb] * cs;
#pragma unroll
      for (int i = 0; i < 18; ++i) {
        if (i < 16) sacc[kt][qb][i] = __builtin_amdgcn_exp2f(fmaf(sacc[kt][qb][i], cs, nm));
        if (i >= 2) asm volatile("v_add_f32 %0, %0, %1" : "+v"(lsum[qb]) : "v"(sacc[kt][qb][i - 2]));
        if (i >= 2 && (i & 1)) pr[qb][(i - 2) >> 3][((i - 2) >> 1) & 3] = pk2(sacc[kt][qb][i - 3], sacc[kt][qb][i - 2]);
        SB();
      }
      pin(pr[qb][0]); pin(pr[qb][1]);
    }
  };
  auto put_p = [&](int kt) AI {
#pragma unroll
    for (int qb = 0; qb < 3; ++qb)
#pragma unroll
      for (int sx = 0; sx < 2; ++sx) sts128((kt == 0 ? PA : PB) + dt * 6 * KB + l16 + (sx * 3 + qb) * KB, pr[qb][sx]);
  };
  const char* wob = wo + (size_t)(4 * g) * 128 * 1280 + c4 * 320;
  const unsigned wa0 = u * 1280 + (((lane & 15) ^ (u << 2)) * 16) + dt * 32 * 1280, wa1 = wa0 + (5120 - 1024), wa2 = wa0 + 2 * (5120 - 1024), wa3 = wa0 + 3 * (5120 - 1024);
  auto issue_wa = [&](int jj) AI {
    const char* src = wob + (size_t)(((i4 + jj) & 3) * 128) * 1280;
    const unsigned d = (jj & 1) ? S(3) + dt * 8 * KB : (dt == 0 ? S(16) : (dt - 1) * 8 * KB);
    dma4<0>(wa0, wa1, wa2, wa3, src, d);
    dma4<0>(wa0, wa1, wa2, wa3, src + 16 * 1280, d + 4096);
  };
  if (r1 && tA) issue_vh(9, 0);
  exp_tile(0);
  if (r1) {
    if (tA) issue_vh(9, 1);
    cnt_wait(C_REL0); issue_v(13); issue_v(14); issue_v(15);
    waitvm<14>();
    bar();
  }
  put_p(0);
  exp_tile(1);
#pragma unroll
  for (int qb = 0; qb < 3; ++qb) {
    const float s = xhalf_sum(lsum[qb]);
    if (hh == 0) *(LAS float*)(size_t)(L_SM + qb * 1024 + mxo + w * 4) = s;
  }
  if (r1) { cnt_wait(C_REL0 + 1); issue_wa(0); waitvm<14>(); }
  STAMP(4);
  bar();
  STAMP(5);
  put_p(1);

  const unsigned v_off = (4 * hh + q4) * 256 + (((4 * dt + 2 * blk + (p4 >> 1)) ^ (q4 << 2)) * 16) + 8 * (p4 & 1);
  const unsigned tag = __builtin_amdgcn_readfirstlane(*(const LAS unsigned*)(size_t)L_TAG);
  f32x16 acc[3] = {};
  if (!r1) {
    f32x16 oa[2][3] = {};
    bf16x8 vf[4][2], pf[2][2][3];
    auto rd_v = [&](int j) AI {
#pragma unroll
      for (int sx = 0; sx < 2; ++sx) vf[j & 3][sx] = ldstr(vslot(j) + v_off + sx * 4096, vslot(j) + v_off + sx * 4096 + 2048);
    };
    auto rd_p = [&](int j) AI {
#pragma unroll
      for (int sx = 0; sx < 2; ++sx)
#pragma unroll
        for (int qb = 0; qb < 3; ++qb) pf[j & 1][sx][qb] = lds128((((j >> 2) & 1) ? PB : PA) + ((j & 3) * 6 + sx * 3 + qb) * KB + l16);
    };
    auto mma_pv = [&](int j) AI {
#pragma unroll
      for (int sx = 0; sx < 2; ++sx)
#pragma unroll
        for (int qb = 0; qb < 3; ++qb) oa[j >> 3][qb] = MFMA(vf[j & 3][sx], pf[j & 1][sx][qb], oa[j >> 3][qb]);
    };
    auto pv_quarter = [&](int q) AI {
      rd_v(4 * q); rd_p(4 * q); rd_v(4 * q + 1); rd_v(4 * q + 2);
      if (q > 0) { mma_pv(4 * q - 1); ilv<6, 3>(); } else SB();
      rd_v(4 * q + 3); rd_p(4 * q + 1); mma_pv(4 * q); ilv<6, 2>();
      rd_p(4 * q + 2); mma_pv(4 * q + 1); ilv<6, 1>();
      asm volatile("s_waitcnt lgkmcnt(6)" ::: "memory"); cnt_add(C_REL0 + q); SB();
      rd_p(4 * q + 3); mma_pv(4 * q + 2); ilv<6, 1>();
    };
    pv_quarter(0); bar(); SB();
    pv_quarter(1); bar(); SB();
    STAMP(6);
    pv_quarter(2);
    float sc0[3], sc1[3];
#pragma unroll
    for (int qb = 0; qb < 3; ++qb) {
      const float m = fmaxf(m0[qb], max4(qb, 1));
      const float al = __builtin_amdgcn_exp2f((m0[qb] - m) * cs);
      f32x4 l0 = *(const LAS f32x4*)(size_t)(L_SM + qb * 1024 + mxo);
      f32x4 l1 = *(const LAS f32x4*)(size_t)(L_SM + qb * 1024 + mxo + 16);
      const float l = al * ((l0[0] + l0[1]) + (l0[2] + l0[3])) + ((l1[0] + l1[1]) + (l1[2] + l1[3]));
      sc1[qb] = 1.0f / l; sc0[qb] = al * sc1[qb];
    }
    bar(); SB();
    STAMP(7);
    pv_quarter(3);
#pragma unroll
    for (int qb = 0; qb < 3; ++qb)
#pragma unroll
      for (int i = 0; i < 16; ++i) oa[0][qb][i] *= sc0[qb];
    mma_pv(15);
    const char* obase = ws + WS_O + (size_t)(b * NH + 4 * g) * 24576;
    const __amdgpu_buffer_rsrc_t ors = __builtin_amdgcn_make_buffer_rsrc((char*)obase, 0, 4 * 24576, 0x00020000);
    cnt_wait(C_REL0 + 3);
#pragma unroll
    for (int qb = 0; qb < 3; ++qb) {
#pragma unroll
      for (int i = 0; i < 16; ++i) oa[1][qb][i] = fmaf(oa[1][qb][i], sc1[qb], oa[0][qb][i]);
#pragma unroll
      for (int sx = 0; sx < 2; ++sx) {
        const u32x4 o = pack8(oa[1][qb], sx);
        st_sc1(ors, i4 * 24576 + (qb * 8 + sx) * KB + dt * 2 * KB + l16, o);
        sts128(oimg(0) + (qb * 8 + sx) * KB + dt * 2 * KB + l16, o);
      }
    }
    waitlds(); cnt_add(C_O0);
    STAMP(8);
    waitvm<0>();
    GAS unsigned* fl = (GAS unsigned*)(ws + WS_F1 + (size_t)(b * 8 + g) * 64);
    if (lane == 0) __hip_atomic_store(fl + dt * 4 + i4, tag, RLX_AGENT);
    STAMP(9);
    if (w == 3) {
      unsigned pend = 0xEu;
      cnt_wait(C_O0);
      NOUNROLL
      for (unsigned spin = 0; pend && spin < SPIN_CAP; ++spin) {
        const unsigned fv = __hip_atomic_load(fl + (lane & 12) + ((i4 + lane) & 3), RLX_AGENT);
        const unsigned m = (unsigned)__ballot(fv == tag);
        const unsigned rdy = m & (m >> 4) & (m >> 8) & (m >> 12) & pend;
        if (!rdy) { __builtin_amdgcn_s_sleep(1); continue; }
        NOUNROLL
        for (unsigned t = rdy; t; t &= t - 1) {
          const unsigned jj = __builtin_ctz(t);
          if (jj >= 2) cnt_wait(C_FREEX + jj - 2);
          const char* src = obase + ((i4 + jj) & 3) * 24576;
          const unsigned d = ((OIMG_TAB >> (4 * jj)) & 15u) << 13;
          NOUNROLL
          for (int i = 0; i < 6; ++i) dma4<2>(l16, l16, l16, l16, src + i * 4096, d + i * 4096);
        }
        waitvm<0>();
        NOUNROLL
        for (unsigned t = rdy; t; t &= t - 1) cnt_add(__builtin_ctz(t), 4);
        pend &= ~rdy;
      }
      if (i4 == 0 && lane == 0) __hip_atomic_store((GAS unsigned*)(ws + WS_EPOCH + (b * 8 + g) * 64), tag, RLX_AGENT);
    } else {
      const unsigned w_off = (4 * hh + q4) * 64 + (2 * blk + (p4 >> 1)) * 16 + 8 * (p4 & 1);
      cnt_wait(C_WOB);
      NOUNROLL
      for (int jj = 0; jj < 4; ++jj) {
        cnt_wait(jj);
        const unsigned wb = (jj == 0 ? S(15) : S(16) + jj * 8 * KB) + w_off;
        const unsigned ob = (((OIMG_TAB >> (4 * jj)) & 15u) << 13) + dt * 8 * KB + l16;
        bf16x8 af[8], of[8];
#pragma unroll
        for (int ds = 0; ds < 8; ++ds) { af[ds] = ldstr(wb + ds * KB, wb + ds * KB + 512); of[ds] = lds128(ob + ds * KB); }
#pragma unroll
        for (int ds = 0; ds < 8; ++ds) acc[0] = MFMA(af[ds], of[ds], acc[0]);
      }
    }
  } else {
    bf16x8 wf[4][8];
    auto pull_wa = [&](int jj) AI {
#pragma unroll
      for (int ds = 0; ds < 8; ++ds) { const unsigned a = wset(jj & 1, ds >> 1) + v_off + (ds & 1) * 4096; wf[jj][ds] = ldstr(a, a + 2048); }
    };
    auto mma_o = [&](int jj) AI {
      bf16x8 of[3][3];
#pragma unroll
      for (int ds = 0; ds < 2; ++ds)
#pragma unroll
        for (int qb = 0; qb < 3; ++qb) of[ds][qb] = lds128(oimg(jj) + (qb * 8 + ds) * KB + l16);
      SB();
#pragma unroll
      for (int ds = 0; ds < 8; ++ds) {
        if (ds < 6) {
#pragma unroll
          for (int qb = 0; qb < 3; ++qb) of[(ds + 2) % 3][qb] = lds128(oimg(jj) + (qb * 8 + ds + 2) * KB + l16);
        }
#pragma unroll
        for (int qb = 0; qb < 3; ++qb) acc[qb] = MFMA(wf[jj][ds], of[ds % 3][qb], acc[qb]);
        ilv<3, 1>();
      }
    };
    cnt_wait(C_REL0 + 2); issue_wa(1);
    waitvm<16>();
    bar();
    STAMP(6);
    {
      const unsigned wb0 = (lane >> 2) * 1280 + (lane & 3) * 16 + 256, wb1 = wb0 + (20480 - 1024), wb2 = wb0 + 2 * (20480 - 1024), wb3 = wb0 + 3 * (20480 - 1024);
      const char* src = wob + (size_t)(((i4 + dt) & 3) * 128) * 1280;
      const unsigned d = dt == 0 ? S(15) : S(16) + dt * 8 * KB;
      dma4<0>(wb0, wb1, wb2, wb3, src, d);
      dma4<0>(wb0, wb1, wb2, wb3, src + 64 * 1280, d + 4096);
    }
    waitvm<8>(); cnt_add(C_L01); cnt_wait(C_L01);
    pull_wa(0); pull_wa(1); waitlds(); cnt_add(C_P01); cnt_wait(C_P01);
    STAMP(7);
    issue_wa(2); issue_wa(3);
    waitvm<16>(); cnt_add(C_WOB);
    waitvm<8>(); cnt_add(C_L2); cnt_wait(C_L2); pull_wa(2); waitlds(); cnt_add(C_FREEX);
    waitvm<0>(); cnt_add(C_L3); cnt_wait(C_L3); pull_wa(3); waitlds(); cnt_add(C_FREEY);
    STAMP(8);
    cnt_wait(0); mma_o(0);
    STAMP(9);
    cnt_wait(1); mma_o(1);
    STAMP(10);
    cnt_wait(2); mma_o(2);
    STAMP(11);
    cnt_wait(3); mma_o(3);
  }
  STAMP(12);
  if (w != 3) {
    const __amdgpu_buffer_rsrc_t prs = __builtin_amdgcn_make_buffer_rsrc(ws + WS_P + (size_t)(b * 4 + c4) * PBYTES, 0, PBYTES, 0x00020000);
    const unsigned so = (((r >> 2) * 8 + w) * 6) * KB + (g * 8 + (r & 3) + 4 * hh) * 16;
#pragma unroll
    for (int t = 0; t < 3; ++t) if (t == 0 || r1) {
#pragma unroll
      for (int uu = 0; uu < 2; ++uu) st_sc1(prs, so + (t * 2 + uu) * KB, pack8(acc[t], uu));
    }
  }
}

__global__ __launch_bounds__(512) void k_reduce(const char* __restrict__ ws, char* __restrict__ out) {
  const int g = blockIdx.x & 7, idx = blockIdx.x >> 3, b = idx >> 2, c4 = idx & 3;
  const int lane = threadIdx.x & 63, w = __builtin_amdgcn_readfirstlane(threadIdx.x >> 6), dt = w & 3;
  const bool r1 = w >= 4;
  if (w == 3) return;
  const char* pb = ws + WS_P + (size_t)(b * 4 + c4) * PBYTES + ((g * 8 + w) * 6) * KB + lane * 16;
  u32x4 t[3][2];
#pragma unroll
  for (int tt = 0; tt < 3; ++tt)
#pragma unroll
    for (int uu = 0; uu < 2; ++uu) t[tt][uu] = *(const u32x4*)(pb + ((r1 ? tt : 0) * 2 + uu) * KB);
  char* orow = out + ((size_t)(b * SQ + (r1 ? 0 : 32 * dt) + 4 * g + (lane & 3)) * DM + 160 * c4 + (r1 ? 32 * dt : 128) + 8 * (lane >> 4) + (lane & 4)) * 2;
#pragma unroll
  for (int tt = 0; tt < 3; ++tt) if (tt == 0 || r1) {
    float xx[16], y[8], z[4];
#pragma unroll
    for (int i = 0; i < 8; ++i) { const unsigned d = t[tt][i >> 2][i & 3]; xx[2 * i] = __uint_as_float(d << 16); xx[2 * i + 1] = __uint_as_float(d & 0xffff0000u); }
#pragma unroll
    for (int i = 0; i < 8; ++i) { auto q = __builtin_amdgcn_permlane32_swap(__float_as_uint(xx[i]), __float_as_uint(xx[i + 8]), false, false); y[i] = __uint_as_float(q[0]) + __uint_as_float(q[1]); }
#pragma unroll
    for (int i = 0; i < 4; ++i) { auto q = __builtin_amdgcn_permlane16_swap(__float_as_uint(y[i]), __float_as_uint(y[i + 4]), false, false); z[i] = __uint_as_float(q[0]) + __uint_as_float(q[1]); }
#pragma unroll
    for (int i = 0; i < 4; ++i) z[i] += __uint_as_float(__builtin_amdgcn_update_dpp(0u, __float_as_uint(z[i]), 0x128, 0xf, 0xf, false));
    const u32x2 o = {pk2(z[0], z[1]), pk2(z[2], z[3])};
    if ((lane & 8) == 0) *(u32x2*)(orow + (size_t)tt * 32 * DM * 2) = o;
  }
  asm volatile(".Lk2_end:");
}

extern "C" void kernel_launch(void* const* d_in, const int* in_sizes, int n_in, void* d_out, int out_size, void* d_ws, size_t ws_size,
                              hipStream_t stream) {
  (void)hipFuncSetAttribute((const void*)k_fused, hipFuncAttributeMaxDynamicSharedMemorySize, LDS_BYTES);
  hipLaunchKernelGGL(k_fused, dim3(256), dim3(512), LDS_BYTES, stream, (const char*)d_in[0], (const char*)d_in[1], (const char*)d_in[2],
                     (const char*)d_in[3], (const char*)d_in[4], (char*)d_ws, (char*)d_out);
  hipLaunchKernelGGL(k_reduce, dim3(256), dim3(512), 0, stream, (const char*)d_ws, (char*)d_out);
}
```

```cpp
#include <hip/hip_runtime.h>
#include <hip/hip_bf16.h>

typedef __attribute__((ext_vector_type(8))) __bf16 bf16x8;
typedef __attribute__((ext_vector_type(4))) __bf16 bf16x4;
typedef __attribute__((ext_vector_type(2))) __bf16 bf16x2;
typedef __attribute__((ext_vector_type(16))) float f32x16;
typedef __attribute__((ext_vector_type(4))) float f32x4;
typedef __attribute__((ext_vector_type(2))) float f32x2;
typedef __attribute__((ext_vector_type(4))) unsigned u32x4;
typedef __attribute__((ext_vector_type(2))) unsigned u32x2;
#define LAS __attribute__((address_space(3)))
#define GAS __attribute__((address_space(1)))
#define DI __device__ __forceinline__
#define AI __attribute__((always_inline))
#define MFMA(a, b, c) __builtin_amdgcn_mfma_f32_32x32x16_bf16((a), (b), (c), 0, 0, 0)
#define SB() __builtin_amdgcn_sched_barrier(0)
#define NOUNROLL _Pragma("clang loop unroll(disable)")

#define STAMP(i) do {} while (0)

constexpr int SQ = 96, DM = 640, NH = 32, SK = 512;
constexpr unsigned KB = 1024;
constexpr unsigned LDS_BYTES = 163840;

constexpr size_t WS_EPOCH = 0;
constexpr size_t WS_F1 = 4096;
constexpr size_t WS_F2 = 16384;
constexpr size_t WS_O = 1u << 20;
constexpr size_t WS_P = 8u << 20;
constexpr unsigned PBYTES = 8 * 8 * 3 * 2 * 1024;

#define DMA_PRE "s_mov_b32 m0, %0\n\ts_nop 0\n\t"
#define DMA4(SUF) DMA_PRE "global_load_lds_dwordx4 %2, %1" SUF "\n\tglobal_load_lds_dwordx4 %3, %1 offset:1024" SUF \
                  "\n\tglobal_load_lds_dwordx4 %4, %1 offset:2048" SUF "\n\tglobal_load_lds_dwordx4 %5, %1 offset:3072" SUF
template <int POL> DI void dma4(unsigned v0, unsigned v1, unsigned v2, unsigned v3, const void* sbase, unsigned lds) {
  lds = __builtin_amdgcn_readfirstlane(lds);
  if constexpr (POL == 1) asm volatile(DMA4(" nt") :: "s"(lds), "s"(sbase), "v"(v0), "v"(v1), "v"(v2), "v"(v3) : "memory");
  else if constexpr (POL == 2) asm volatile(DMA4(" sc1") :: "s"(lds), "s"(sbase), "v"(v0), "v"(v1), "v"(v2), "v"(v3) : "memory");
  else asm volatile(DMA4("") :: "s"(lds), "s"(sbase), "v"(v0), "v"(v1), "v"(v2), "v"(v3) : "memory");
}
DI void dma3(unsigned v0, unsigned v1, unsigned v2, const void* sbase, unsigned lds) {
  lds = __builtin_amdgcn_readfirstlane(lds);
  asm volatile(DMA_PRE "global_load_lds_dwordx4 %2, %1\n\tglobal_load_lds_dwordx4 %3, %1 offset:1024\n\tglobal_load_lds_dwordx4 %4, %1 offset:2048"
               :: "s"(lds), "s"(sbase), "v"(v0), "v"(v1), "v"(v2) : "memory");
}
DI void dma2nt(unsigned v0, const void* sbase, unsigned lds) {
  lds = __builtin_amdgcn_readfirstlane(lds);
  asm volatile(DMA_PRE "global_load_lds_dwordx4 %2, %1 nt\n\tglobal_load_lds_dwordx4 %2, %1 offset:1024 nt" :: "s"(lds), "s"(sbase), "v"(v0) : "memory");
}
template <int N> DI void waitvm() { asm volatile("s_waitcnt vmcnt(%0)" ::"i"(N) : "memory"); }
DI void bar() { asm volatile("s_waitcnt lgkmcnt(0)\n\ts_barrier" ::: "memory"); }
DI void waitlds() { asm volatile("s_waitcnt lgkmcnt(0)" ::: "memory"); }
DI unsigned pk2(float a, float b) { f32x2 t = {a, b}; bf16x2 p = __builtin_convertvector(t, bf16x2); return __builtin_bit_cast(unsigned, p); }
DI bf16x8 lds128(unsigned a) { return *(const LAS bf16x8*)(size_t)a; }
DI void sts128(unsigned a, u32x4 v) { *(LAS u32x4*)(size_t)a = v; }
DI bf16x8 ldstr(unsigned a0, unsigned a1) {
  bf16x4 a = __builtin_amdgcn_ds_read_tr16_b64_v4bf16((LAS bf16x4*)(size_t)a0);
  bf16x4 b = __builtin_amdgcn_ds_read_tr16_b64_v4bf16((LAS bf16x4*)(size_t)a1);
  return __builtin_shufflevector(a, b, 0, 1, 2, 3, 4, 5, 6, 7);
}
DI float xhalf_max(float v) { auto r = __builtin_amdgcn_permlane32_swap(__float_as_uint(v), __float_as_uint(v), false, false); return fmaxf(__uint_as_float(r[0]), __uint_as_float(r[1])); }
DI float xhalf_sum(float v) { auto r = __builtin_amdgcn_permlane32_swap(__float_as_uint(v), __float_as_uint(v), false, false); return __uint_as_float(r[0]) + __uint_as_float(r[1]); }
DI u32x4 pack8(const f32x16& a, int s) {
  u32x4 o = {pk2(a[8 * s], a[8 * s + 1]), pk2(a[8 * s + 2], a[8 * s + 3]), pk2(a[8 * s + 4], a[8 * s + 5]), pk2(a[8 * s + 6], a[8 * s + 7])};
  return o;
}
DI void pin(u32x4& x) { asm volatile("" : "+v"(x)); }
template <int NM, int ND> DI void ilv() {
#pragma unroll
  for (int i = 0; i < NM; ++i) { __builtin_amdgcn_sched_group_barrier(0x008, 1, 0); __builtin_amdgcn_sched_group_barrier(0x100, ND, 0); }
  __builtin_amdgcn_sched_barrier(0);
}
DI void st_sc1(__amdgpu_buffer_rsrc_t rs, unsigned off, u32x4 v) { __builtin_amdgcn_raw_buffer_store_b128(v, rs, off, 0, 16); }
DI u32x4 ld_sc1(__amdgpu_buffer_rsrc_t rs, unsigned off) { return __builtin_amdgcn_raw_buffer_load_b128(rs, off, 0, 16); }
#define RLX_AGENT __ATOMIC_RELAXED, __HIP_MEMORY_SCOPE_AGENT
constexpr unsigned SPIN_CAP = 1u << 18;

constexpr unsigned S(int i) { return 8 * KB * i; }
constexpr unsigned area(int a) { return a == 0 ? S(12) : a == 1 ? S(16) : a == 2 ? S(8) : a == 3 ? S(0) : S(4); }
constexpr unsigned AREA_TAB = 0x10243u;
constexpr unsigned QIMG = S(8), PB = S(8), PA = S(17), MISC = S(11);
constexpr unsigned L_MX = MISC, L_SM = MISC + 3072, L_TAG = MISC + 6144, L_CNT = MISC + 6400;
constexpr unsigned vslot(int j) { return j < 5 ? S(12 + j) : j < 13 ? S(j - 5) : S(12 + j - 13); }
constexpr unsigned oimg(int jj) { return jj == 0 ? S(12) : jj == 1 ? S(8) : jj == 2 ? S(0) : S(4); }
constexpr unsigned OIMG_TAB = 0x408Cu;
constexpr unsigned wobs(int jj) { return jj == 0 ? S(15) : S(16 + jj); }
constexpr unsigned wset(int set, int t) { return set == 0 ? (t == 0 ? S(16) : S(t - 1)) : S(3 + t); }
enum { C_O0 = 0, C_FREEX = 4, C_FREEY = 5, C_WOB = 6, C_REL0 = 7, C_L01 = 11, C_P01 = 12, C_L2 = 13, C_L3 = 14 };
DI void cnt_add(unsigned idx, unsigned n = 1) {
  unsigned long long keep; const unsigned a = L_CNT + idx * 4;
  asm volatile("s_mov_b64 %0, exec\n\ts_mov_b64 exec, 1\n\tds_add_u32 %1, %2\n\ts_mov_b64 exec, %0" : "=&s"(keep) : "v"(a), "v"(n) : "memory");
}
DI void cnt_wait(unsigned idx) {
  const unsigned a = L_CNT + idx * 4; unsigned v, s, n = 1u << 24;
  asm volatile("1:\n\tds_read_b32 %0, %3\n\ts_waitcnt lgkmcnt(0)\n\tv_readfirstlane_b32 %1, %0\n\ts_cmp_ge_u32 %1, 4\n\ts_cbranch_scc1 2f\n\t"
               "s_sleep 1\n\ts_sub_u32 %2, %2, 1\n\ts_cmp_lg_u32 %2, 0\n\ts_cbranch_scc1 1b\n2:"
               : "=&v"(v), "=&s"(s), "+s"(n) : "v"(a) : "memory", "scc");
}

__global__ __launch_bounds__(512, 2) void k_fused(const char* __restrict__ x, const char* __restrict__ kc, const char* __restrict__ vc,
                                                   const char* __restrict__ wq, const char* __restrict__ wo, char* __restrict__ ws, char* __restrict__ out) {
  const int g = blockIdx.x & 7, idx = blockIdx.x >> 3, b = idx >> 2, i4 = idx & 3, h = 4 * g + i4;
  const int c4 = (i4 + (b >> 1)) & 3;
  const bool tA = (i4 & 1) == 0;
  const int tid = threadIdx.x, lane = tid & 63;
  const int w = __builtin_amdgcn_readfirstlane(tid >> 6), dt = w & 3;
  const bool r1 = w >= 4;
  const int r = lane & 31, hh = lane >> 5;
  const int blk = (lane >> 4) & 1, q4 = (lane & 15) >> 2, p4 = lane & 3;
  const unsigned u = lane >> 4, l16 = lane * 16;
  STAMP(0);
  const char* kb = kc + ((size_t)(b * NH + h) * SK) * 256;
  const char* vb = vc + ((size_t)(b * NH + h) * SK) * 256;
  const unsigned vo_tr = u * 256 + (((lane & 15) ^ (u << 2)) * 16);
  const unsigned vqv = vo_tr + dt * 8192, vtv = vo_tr + dt * 2048;
  const unsigned k0v = u * 256 + (((lane & 15) ^ u) * 16) + dt * 8192, k1v = k0v ^ 64, k2v = k0v ^ 128, k3v = k0v ^ 192;
  auto karea = [&](int q) AI { return (q < 2 && tA) ? area(q) : area(q + 1); };
  auto issue_kq = [&](int q) AI {
    const unsigned d = karea(q) + dt * 8 * KB;
    dma4<1>(k0v, k1v, k2v, k3v, kb + q * 32768, d);
    dma4<1>(k0v, k1v, k2v, k3v, kb + q * 32768 + 4096, d + 4096);
  };
  auto issue_v = [&](int j) AI { dma2nt(vtv, vb + j * 8192, vslot(j) + dt * 2 * KB); };
  auto issue_vh = [&](int j0, int half) AI { dma4<1>(vqv, vqv, vqv, vqv, vb + j0 * 8192 + half * 4096, vslot(j0) + dt * 8 * KB + half * 4096); };
  bf16x8 kf[2][8];
  const unsigned kfo = dt * 8 * KB + r * 256 + ((hh ^ (r & 15)) * 16);
  auto pull_k = [&](int kt) AI {
    const unsigned slot = (r1 ? area(3 + kt) : karea(kt)) + kfo;
#pragma unroll
    for (int s = 0; s < 8; ++s) kf[kt][s] = lds128(slot ^ (s * 32));
  };

  if (!r1) {
    const unsigned epoch = __hip_atomic_load((GAS unsigned*)(ws + WS_EPOCH + (b * 8 + g) * 64), RLX_AGENT);
    {
      unsigned long long pc; unsigned len;
      asm volatile("s_getpc_b64 %0\n.Lk1_here:\n\ts_mov_b32 %1, .Lk2_end-.Lk1_here" : "=s"(pc), "=s"(len));
      const unsigned off = (idx * 4 + dt) * 512 + (lane & 7) * 64;
      unsigned sink = 0;
      if (lane < 8 && off < len) sink = *(const GAS unsigned*)(pc + off);
      if (0 && lane >= 8 && lane < 8 + 12) sink = *(const GAS unsigned*)(ws + (size_t)(lane - 8) * (2u << 20) + (1u << 20) - 4096 + idx * 64);
      asm volatile("" :: "v"(sink));
    }
    if (!tA) { bar(); pull_k(0); bar(); pull_k(1); }
    STAMP(1);
    f32x16 qacc[3] = {};
    const int p4s = ((p4 & 1) << 1) | (p4 >> 1);
    const unsigned a_off = (8 * hh + q4) * 256 + (((4 * dt + 2 * blk + (p4s >> 1)) ^ (q4 << 2)) * 16) + 8 * (p4s & 1);
    const unsigned x_off = r * 128 + 16 * KB + ((hh ^ ((r >> 1) & 7)) * 16);
    bf16x8 fa[4] = {}, fb[4][3] = {};
    auto read_q = [&](unsigned base, int ks) AI {
      fa[ks] = ldstr(base + a_off + ks * 4096, base + a_off + ks * 4096 + 1024);
#pragma unroll
      for (int j = 0; j < 3; ++j) fb[ks][j] = lds128(base + (x_off ^ (ks * 32)) + j * 4096);
    };
    auto mma_q = [&](int ks) AI {
#pragma unroll
      for (int j = 0; j < 3; ++j) qacc[j] = MFMA(fa[ks], fb[ks][j], qacc[j]);
    };
    NOUNROLL
    for (int it = 0; it < 2; ++it) {
#pragma unroll
      for (int a = 0; a < 5; ++a) {
        constexpr unsigned dummy = 0; (void)dummy;
        const unsigned base = area(a);
        bar(); SB();
        read_q(base, 0); read_q(base, 1); mma_q(2); ilv<3, 4>();
        read_q(base, 2); mma_q(3); ilv<3, 2>();
        read_q(base, 3); mma_q(0); ilv<3, 2>();
        mma_q(1); SB();
      }
    }
    mma_q(2); mma_q(3);
#pragma unroll
    for (int j = 0; j < 3; ++j)
#pragma unroll
      for (int sx = 0; sx < 2; ++sx) sts128(QIMG + (j * 8 + sx) * KB + dt * 2 * KB + l16, pack8(qacc[j], sx));
    if (tid < 16) *(LAS unsigned*)(size_t)(L_CNT + tid * 4) = 0u;
    if (tid == 0) *(LAS unsigned*)(size_t)L_TAG = epoch * 1664525u + 1013904223u;
    bar();
    STAMP(2);
  } else {
    const char* xb = x + (size_t)b * SQ * DM * 2;
    const char* wqh = wq + (size_t)h * 256;
    const unsigned vo_x = (lane >> 3) * 1280 + (((lane & 7) ^ u) * 16);
    const unsigned wq0 = u * 8192 + (((lane & 15) ^ (u << 2)) * 16) + dt * 16 * 8192, wq1 = wq0 + (32768 - 1024), wq2 = wq0 + 2 * (32768 - 1024), wq3 = wq0 + 3 * (32768 - 1024);
    const unsigned xb0 = (vo_x ^ ((dt & 1) << 6)) + dt * 3 * 10240, xb1 = (vo_x ^ (((dt + 1) & 1) << 6)) + dt * 3 * 10240 + (10240 - 1024), xb2 = xb0 + 2 * (10240 - 1024);
    const int rot = (b + 3 * i4 + g) % 10;
    auto issue_q = [&](int s0) AI {
      int s = s0 + rot; if (s >= 10) s -= 10;
      const unsigned base = ((AREA_TAB >> (4 * (s0 >= 5 ? s0 - 5 : s0))) & 15u) << 15;
      dma4<0>(wq0, wq1, wq2, wq3, wqh + (size_t)s * (64 * 8192), base + 4 * dt * KB);
      dma3(xb0, xb1, xb2, xb + s * 128, base + 16 * KB + 3 * dt * KB);
    };
    if (tA) {
      issue_q(0); issue_q(1); issue_q(2); waitvm<14>(); bar();
    } else {
      issue_kq(0); issue_kq(1); issue_kq(2);
      waitvm<16>(); bar(); issue_kq(3);
      waitvm<16>(); bar(); issue_q(0);
      waitvm<15>(); pull_k(0); issue_q(1);
      waitvm<14>(); pull_k(1);
      STAMP(1);
      waitvm<7>(); bar(); issue_q(2);
    }
    NOUNROLL
    for (int s0 = 0; s0 < 7; ++s0) { issue_q(s0 + 3); waitvm<14>(); bar(); }
    if (tA) {
      issue_kq(0); waitvm<15>(); bar();
      issue_kq(1); waitvm<16>(); bar();
      issue_kq(2); waitvm<16>(); bar();
      issue_kq(3); waitvm<16>(); bar();
    } else {
      waitvm<7>(); bar();
      issue_vh(0, 0); issue_vh(0, 1); waitvm<8>(); bar();
      issue_v(4); bar();
      issue_vh(5, 0); issue_vh(5, 1);
    }
    STAMP(2);
    bar();
  }

  f32x16 sacc[2][3] = {};
  {
    bf16x8 qf[2][6];
    auto rd_q6 = [&](int st) AI {
#pragma unroll
      for (int i = 0; i < 6; ++i) qf[st & 1][i] = lds128(QIMG + ((i % 3) * 8 + 2 * st + i / 3) * KB + l16);
    };
    auto s_tile = [&](int kt) AI {
      rd_q6(0); SB();
#pragma unroll
      for (int st = 0; st < 4; ++st) {
        if (st < 3) rd_q6(st + 1);
#pragma unroll
        for (int i = 0; i < 6; ++i) sacc[kt][i % 3] = MFMA(kf[kt][2 * st + i / 3], qf[st & 1][i], sacc[kt][i % 3]);
        ilv<6, 1>();
      }
    };
    if (tA) {
      if (r1) waitvm<8>(); pull_k(0); if (r1) issue_vh(0, 0);
      s_tile(0);
      if (r1) { issue_vh(0, 1); waitvm<8>(); } else bar(); pull_k(1); if (r1) issue_v(4);
      s_tile(1);
    } else {
      rd_q6(0); SB();
#pragma unroll
      for (int st = 0; st < 4; ++st) {
        if (st < 3) rd_q6(st + 1);
#pragma unroll
        for (int i = 0; i < 6; ++i) {
          sacc[0][i % 3] = MFMA(kf[0][2 * st + i / 3], qf[st & 1][i], sacc[0][i % 3]);
          sacc[1][i % 3] = MFMA(kf[1][2 * st + i / 3], qf[st & 1][i], sacc[1][i % 3]);
        }
        ilv<6, 1>();
      }
    }
  }
  const unsigned mxo = r * 32;
#pragma unroll
  for (int qb = 0; qb < 3; ++qb) {
    float m = sacc[0][qb][0];
#pragma unroll
    for (int kt = 0; kt < 2; ++kt)
#pragma unroll
      for (int i = 0; i < 16; ++i) m = fmaxf(m, sacc[kt][qb][i]);
    m = xhalf_max(m);
    if (hh == 0) *(LAS float*)(size_t)(L_MX + qb * 1024 + mxo + w * 4) = m;
  }
  if (r1) {
    if (tA) { issue_vh(5, 0); issue_vh(5, 1); waitvm<10>(); } else { issue_vh(9, 0); issue_vh(9, 1); waitvm<18>(); }
  }
  bar();
  STAMP(3);
  const float cs = 0.08838834764831845f * 1.4426950408889634f;
  auto max4 = [&](int qb, int half) AI {
    f32x4 m = *(const LAS f32x4*)(size_t)(L_MX + qb * 1024 + mxo + half * 16);
    return fmaxf(fmaxf(m[0], m[1]), fmaxf(m[2], m[3]));
  };
  float m0[3], lsum[3] = {0.f, 0.f, 0.f};
#pragma unroll
  for (int qb = 0; qb < 3; ++qb) m0[qb] = fmaxf(max4(qb, 0), max4(qb, r1));
  u32x4 pr[3][2];
  auto exp_tile = [&](int kt) AI {
#pragma unroll
    for (int qb = 0; qb < 3; ++qb) {
      const float nm = -m0[qb] * cs;
#pragma unroll
      for (int i = 0; i < 18; ++i) {
        if (i < 16) sacc[kt][qb][i] = __builtin_amdgcn_exp2f(fmaf(sacc[kt][qb][i], cs, nm));
        if (i >= 2) asm volatile("v_add_f32 %0, %0, %1" : "+v"(lsum[qb]) : "v"(sacc[kt][qb][i - 2]));
        if (i >= 2 && (i & 1)) pr[qb][(i - 2) >> 3][((i - 2) >> 1) & 3] = pk2(sacc[kt][qb][i - 3], sacc[kt][qb][i - 2]);
        SB();
      }
      pin(pr[qb][0]); pin(pr[qb][1]);
    }
  };
  auto put_p = [&](int kt) AI {
#pragma unroll
    for (int qb = 0; qb < 3; ++qb)
#pragma unroll
      for (int sx = 0; sx < 2; ++sx) sts128((kt == 0 ? PA : PB) + dt * 6 * KB + l16 + (sx * 3 + qb) * KB, pr[qb][sx]);
  };
  const char* wob = wo + (size_t)(4 * g) * 128 * 1280 + c4 * 320;
  const unsigned wa0 = u * 1280 + (((lane & 15) ^ (u << 2)) * 16) + dt * 32 * 1280, wa1 = wa0 + (5120 - 1024), wa2 = wa0 + 2 * (5120 - 1024), wa3 = wa0 + 3 * (5120 - 1024);
  auto issue_wa = [&](int jj) AI {
    const char* src = wob + (size_t)(((i4 + jj) & 3) * 128) * 1280;
    const unsigned d = (jj & 1) ? S(3) + dt * 8 * KB : (dt == 0 ? S(16) : (dt - 1) * 8 * KB);
    dma4<0>(wa0, wa1, wa2, wa3, src, d);
    dma4<0>(wa0, wa1, wa2, wa3, src + 16 * 1280, d + 4096);
  };
  if (r1 && tA) issue_vh(9, 0);
  exp_tile(0);
  if (r1) {
    if (tA) issue_vh(9, 1);
    cnt_wait(C_REL0); issue_v(13); issue_v(14); issue_v(15);
    waitvm<14>();
    bar();
  }
  put_p(0);
  exp_tile(1);
#pragma unroll
  for (int qb = 0; qb < 3; ++qb) {
    const float s = xhalf_sum(lsum[qb]);
    if (hh == 0) *(LAS float*)(size_t)(L_SM + qb * 1024 + mxo + w * 4) = s;
  }
  if (r1) { cnt_wait(C_REL0 + 1); issue_wa(0); waitvm<14>(); }
  STAMP(4);
  bar();
  STAMP(5);
  put_p(1);

  const unsigned v_off = (4 * hh + q4) * 256 + (((4 * dt + 2 * blk + (p4 >> 1)) ^ (q4 << 2)) * 16) + 8 * (p4 & 1);
  const unsigned tag = __builtin_amdgcn_readfirstlane(*(const LAS unsigned*)(size_t)L_TAG);
  f32x16 acc[3] = {};
  if (!r1) {
    f32x16 oa[2][3] = {};
    bf16x8 vf[4][2], pf[2][2][3];
    auto rd_v = [&](int j) AI {
#pragma unroll
      for (int sx = 0; sx < 2; ++sx) vf[j & 3][sx] = ldstr(vslot(j) + v_off + sx * 4096, vslot(j) + v_off + sx * 4096 + 2048);
    };
    auto rd_p = [&](int j) AI {
#pragma unroll
      for (int sx = 0; sx < 2; ++sx)
#pragma unroll
        for (int qb = 0; qb < 3; ++qb) pf[j & 1][sx][qb] = lds128((((j >> 2) & 1) ? PB : PA) + ((j & 3) * 6 + sx * 3 + qb) * KB + l16);
    };
    auto mma_pv = [&](int j) AI {
#pragma unroll
      for (int sx = 0; sx < 2; ++sx)
#pragma unroll
        for (int qb = 0; qb < 3; ++qb) oa[j >> 3][qb] = MFMA(vf[j & 3][sx], pf[j & 1][sx][qb], oa[j >> 3][qb]);
    };
    auto pv_quarter = [&](int q) AI {
      rd_v(4 * q); rd_p(4 * q); rd_v(4 * q + 1); rd_v(4 * q + 2);
      if (q > 0) { mma_pv(4 * q - 1); ilv<6, 3>(); } else SB();
      rd_v(4 * q + 3); rd_p(4 * q + 1); mma_pv(4 * q); ilv<6, 2>();
      rd_p(4 * q + 2); mma_pv(4 * q + 1); ilv<6, 1>();
      asm volatile("s_waitcnt lgkmcnt(6)" ::: "memory"); cnt_add(C_REL0 + q); SB();
      rd_p(4 * q + 3); mma_pv(4 * q + 2); ilv<6, 1>();
    };
    pv_quarter(0); bar(); SB();
    pv_quarter(1); bar(); SB();
    STAMP(6);
    pv_quarter(2);
    float sc0[3], sc1[3];
#pragma unroll
    for (int qb = 0; qb < 3; ++qb) {
      const float m = fmaxf(m0[qb], max4(qb, 1));
      const float al = __builtin_amdgcn_exp2f((m0[qb] - m) * cs);
      f32x4 l0 = *(const LAS f32x4*)(size_t)(L_SM + qb * 1024 + mxo);
      f32x4 l1 = *(const LAS f32x4*)(size_t)(L_SM + qb * 1024 + mxo + 16);
      const float l = al * ((l0[0] + l0[1]) + (l0[2] + l0[3])) + ((l1[0] + l1[1]) + (l1[2] + l1[3]));
      sc1[qb] = 1.0f / l; sc0[qb] = al * sc1[qb];
    }
    bar(); SB();
    STAMP(7);
    pv_quarter(3);
#pragma unroll
    for (int qb = 0; qb < 3; ++qb)
#pragma unroll
      for (int i = 0; i < 16; ++i) oa[0][qb][i] *= sc0[qb];
    mma_pv(15);
    const char* obase = ws + WS_O + (size_t)(b * NH + 4 * g) * 24576;
    const __amdgpu_buffer_rsrc_t ors = __builtin_amdgcn_make_buffer_rsrc((char*)obase, 0, 4 * 24576, 0x00020000);
    cnt_wait(C_REL0 + 3);
#pragma unroll
    for (int qb = 0; qb < 3; ++qb) {
#pragma unroll
      for (int i = 0; i < 16; ++i) oa[1][qb][i] = fmaf(oa[1][qb][i], sc1[qb], oa[0][qb][i]);
#pragma unroll
      for (int sx = 0; sx < 2; ++sx) {
        const u32x4 o = pack8(oa[1][qb], sx);
        st_sc1(ors, i4 * 24576 + (qb * 8 + sx) * KB + dt * 2 * KB + l16, o);
        sts128(oimg(0) + (qb * 8 + sx) * KB + dt * 2 * KB + l16, o);
      }
    }
    waitlds(); cnt_add(C_O0);
    STAMP(8);
    waitvm<0>();
    GAS unsigned* fl = (GAS unsigned*)(ws + WS_F1 + (size_t)(b * 8 + g) * 64);
    if (lane == 0) __hip_atomic_store(fl + dt * 4 + i4, tag, RLX_AGENT);
    STAMP(9);
    if (w == 3) {
      unsigned pend = 0xEu;
      cnt_wait(C_O0);
      NOUNROLL
      for (unsigned spin = 0; pend && spin < SPIN_CAP; ++spin) {
        const unsigned fv = __hip_atomic_load(fl + (lane & 12) + ((i4 + lane) & 3), RLX_AGENT);
        const unsigned m = (unsigned)__ballot(fv == tag);
        const unsigned rdy = m & (m >> 4) & (m >> 8) & (m >> 12) & pend;
        if (!rdy) { __builtin_amdgcn_s_sleep(1); continue; }
        NOUNROLL
        for (unsigned t = rdy; t; t &= t - 1) {
          const unsigned jj = __builtin_ctz(t);
          if (jj >= 2) cnt_wait(C_FREEX + jj - 2);
          const char* src = obase + ((i4 + jj) & 3) * 24576;
          const unsigned d = ((OIMG_TAB >> (4 * jj)) & 15u) << 13;
          NOUNROLL
          for (int i = 0; i < 6; ++i) dma4<2>(l16, l16, l16, l16, src + i * 4096, d + i * 4096);
        }
        waitvm<0>();
        NOUNROLL
        for (unsigned t = rdy; t; t &= t - 1) cnt_add(__builtin_ctz(t), 4);
        pend &= ~rdy;
      }
      if (i4 == 0 && lane == 0) __hip_atomic_store((GAS unsigned*)(ws + WS_EPOCH + (b * 8 + g) * 64), tag, RLX_AGENT);
    } else {
      const unsigned w_off = (4 * hh + q4) * 64 + (2 * blk + (p4 >> 1)) * 16 + 8 * (p4 & 1);
      cnt_wait(C_WOB);
      NOUNROLL
      for (int jj = 0; jj < 4; ++jj) {
        cnt_wait(jj);
        const unsigned wb = (jj == 0 ? S(15) : S(16) + jj * 8 * KB) + w_off;
        const unsigned ob = (((OIMG_TAB >> (4 * jj)) & 15u) << 13) + dt * 8 * KB + l16;
        bf16x8 af[8], of[8];
#pragma unroll
        for (int ds = 0; ds < 8; ++ds) { af[ds] = ldstr(wb + ds * KB, wb + ds * KB + 512); of[ds] = lds128(ob + ds * KB); }
#pragma unroll
        for (int ds = 0; ds < 8; ++ds) acc[0] = MFMA(af[ds], of[ds], acc[0]);
      }
    }
  } else {
    bf16x8 wf[4][8];
    auto pull_wa = [&](int jj) AI {
#pragma unroll
      for (int ds = 0; ds < 8; ++ds) { const unsigned a = wset(jj & 1, ds >> 1) + v_off + (ds & 1) * 4096; wf[jj][ds] = ldstr(a, a + 2048); }
    };
    auto mma_o = [&](int jj) AI {
      bf16x8 of[3][3];
#pragma unroll
      for (int ds = 0; ds < 2; ++ds)
#pragma unroll
        for (int qb = 0; qb < 3; ++qb) of[ds][qb] = lds128(oimg(jj) + (qb * 8 + ds) * KB + l16);
      SB();
#pragma unroll
      for (int ds = 0; ds < 8; ++ds) {
        if (ds < 6) {
#pragma unroll
          for (int qb = 0; qb < 3; ++qb) of[(ds + 2) % 3][qb] = lds128(oimg(jj) + (qb * 8 + ds + 2) * KB + l16);
        }
#pragma unroll
        for (int qb = 0; qb < 3; ++qb) acc[qb] = MFMA(wf[jj][ds], of[ds % 3][qb], acc[qb]);
        ilv<3, 1>();
      }
    };
    cnt_wait(C_REL0 + 2); issue_wa(1);
    waitvm<16>();
    bar();
    STAMP(6);
    {
      const unsigned wb0 = (lane >> 2) * 1280 + (lane & 3) * 16 + 256, wb1 = wb0 + (20480 - 1024), wb2 = wb0 + 2 * (20480 - 1024), wb3 = wb0 + 3 * (20480 - 1024);
      const char* src = wob + (size_t)(((i4 + dt) & 3) * 128) * 1280;
      const unsigned d = dt == 0 ? S(15) : S(16) + dt * 8 * KB;
      dma4<0>(wb0, wb1, wb2, wb3, src, d);
      dma4<0>(wb0, wb1, wb2, wb3, src + 64 * 1280, d + 4096);
    }
    waitvm<8>(); cnt_add(C_L01); cnt_wait(C_L01);
    pull_wa(0); pull_wa(1); waitlds(); cnt_add(C_P01); cnt_wait(C_P01);
    STAMP(7);
    issue_wa(2); issue_wa(3);
    waitvm<16>(); cnt_add(C_WOB);
    waitvm<8>(); cnt_add(C_L2); cnt_wait(C_L2); pull_wa(2); waitlds(); cnt_add(C_FREEX);
    waitvm<0>(); cnt_add(C_L3); cnt_wait(C_L3); pull_wa(3); waitlds(); cnt_add(C_FREEY);
    STAMP(8);
    cnt_wait(0); mma_o(0);
    STAMP(9);
    cnt_wait(1); mma_o(1);
    STAMP(10);
    cnt_wait(2); mma_o(2);
    STAMP(11);
    cnt_wait(3); mma_o(3);
  }
  STAMP(12);
  if (w != 3) {
    const __amdgpu_buffer_rsrc_t prs = __builtin_amdgcn_make_buffer_rsrc(ws + WS_P + (size_t)(b * 4 + c4) * PBYTES, 0, PBYTES, 0x00020000);
    const unsigned so = (((r >> 2) * 8 + w) * 6) * KB + (g * 8 + (r & 3) + 4 * hh) * 16;
#pragma unroll
    for (int t = 0; t < 3; ++t) if (t == 0 || r1) {
#pragma unroll
      for (int uu = 0; uu < 2; ++uu) st_sc1(prs, so + (t * 2 + uu) * KB, pack8(acc[t], uu));
    }
  }
}

__global__ __launch_bounds__(512) void k_reduce(const char* __restrict__ ws, char* __restrict__ out) {
  const int g = blockIdx.x & 7, idx = blockIdx.x >> 3, b = idx >> 2, c4 = idx & 3;
  const int lane = threadIdx.x & 63, w = __builtin_amdgcn_readfirstlane(threadIdx.x >> 6), dt = w & 3;
  const bool r1 = w >= 4;
  if (w == 3) return;
  const char* pb = ws + WS_P + (size_t)(b * 4 + c4) * PBYTES + ((g * 8 + w) * 6) * KB + lane * 16;
  u32x4 t[3][2];
#pragma unroll
  for (int tt = 0; tt < 3; ++tt)
#pragma unroll
    for (int uu = 0; uu < 2; ++uu) t[tt][uu] = *(const u32x4*)(pb + ((r1 ? tt : 0) * 2 + uu) * KB);
  char* orow = out + ((size_t)(b * SQ + (r1 ? 0 : 32 * dt) + 4 * g + (lane & 3)) * DM + 160 * c4 + (r1 ? 32 * dt : 128) + 8 * (lane >> 4) + (lane & 4)) * 2;
#pragma unroll
  for (int tt = 0; tt < 3; ++tt) if (tt == 0 || r1) {
    float xx[16], y[8], z[4];
#pragma unroll
    for (int i = 0; i < 8; ++i) { const unsigned d = t[tt][i >> 2][i & 3]; xx[2 * i] = __uint_as_float(d << 16); xx[2 * i + 1] = __uint_as_float(d & 0xffff0000u); }
#pragma unroll
    for (int i = 0; i < 8; ++i) { auto q = __builtin_amdgcn_permlane32_swap(__float_as_uint(xx[i]), __float_as_uint(xx[i + 8]), false, false); y[i] = __uint_as_float(q[0]) + __uint_as_float(q[1]); }
#pragma unroll
    for (int i = 0; i < 4; ++i) { auto q = __builtin_amdgcn_permlane16_swap(__float_as_uint(y[i]), __float_as_uint(y[i + 4]), false, false); z[i] = __uint_as_float(q[0]) + __uint_as_float(q[1]); }
#pragma unroll
    for (int i = 0; i < 4; ++i) z[i] += __uint_as_float(__builtin_amdgcn_update_dpp(0u, __float_as_uint(z[i]), 0x128, 0xf, 0xf, false));
    const u32x2 o = {pk2(z[0], z[1]), pk2(z[2], z[3])};
    if ((lane & 8) == 0) *(u32x2*)(orow + (size_t)tt * 32 * DM * 2) = o;
  }
  asm volatile(".Lk2_end:");
}

extern "C" void kernel_launch(void* const* d_in, const int* in_sizes, int n_in, void* d_out, int out_size, void* d_ws, size_t ws_size,
                              hipStream_t stream) {
  (void)hipFuncSetAttribute((const void*)k_fused, hipFuncAttributeMaxDynamicSharedMemorySize, LDS_BYTES);
  hipLaunchKernelGGL(k_fused, dim3(256), dim3(512), LDS_BYTES, stream, (const char*)d_in[0], (const char*)d_in[1], (const char*)d_in[2],
                     (const char*)d_in[3], (const char*)d_in[4], (char*)d_ws, (char*)d_out);
  hipLaunchKernelGGL(k_reduce, dim3(256), dim3(512), 0, stream, (const char*)d_ws, (char*)d_out);
}
```

```cpp
#include <hip/hip_runtime.h>
#include <hip/hip_bf16.h>

typedef __attribute__((ext_vector_type(8))) __bf16 bf16x8;
typedef __attribute__((ext_vector_type(4))) __bf16 bf16x4;
typedef __attribute__((ext_vector_type(2))) __bf16 bf16x2;
typedef __attribute__((ext_vector_type(16))) float f32x16;
typedef __attribute__((ext_vector_type(4))) float f32x4;
typedef __attribute__((ext_vector_type(2))) float f32x2;
typedef __attribute__((ext_vector_type(4))) unsigned u32x4;
typedef __attribute__((ext_vector_type(2))) unsigned u32x2;
#define LAS __attribute__((address_space(3)))
#define GAS __attribute__((address_space(1)))
#define DI __device__ __forceinline__
#define AI __attribute__((always_inline))
#define MFMA(a, b, c) __builtin_amdgcn_mfma_f32_32x32x16_bf16((a), (b), (c), 0, 0, 0)
#define SB() __builtin_amdgcn_sched_barrier(0)
#define NOUNROLL _Pragma("clang loop unroll(disable)")

#define STAMP(i) do {} while (0)

constexpr int SQ = 96, DM = 640, NH = 32, SK = 512;
constexpr unsigned KB = 1024;
constexpr unsigned LDS_BYTES = 163840;

constexpr size_t WS_EPOCH = 0;
constexpr size_t WS_F1 = 4096;
constexpr size_t WS_F2 = 16384;
constexpr size_t WS_O = 1u << 20;
constexpr size_t WS_P = 8u << 20;
constexpr unsigned PBYTES = 8 * 8 * 3 * 2 * 1024;

#define DMA_PRE "s_mov_b32 m0, %0\n\ts_nop 0\n\t"
#define DMA4(SUF) DMA_PRE "global_load_lds_dwordx4 %2, %1" SUF "\n\tglobal_load_lds_dwordx4 %3, %1 offset:1024" SUF \
                  "\n\tglobal_load_lds_dwordx4 %4, %1 offset:2048" SUF "\n\tglobal_load_lds_dwordx4 %5, %1 offset:3072" SUF
template <int POL> DI void dma4(unsigned v0, unsigned v1, unsigned v2, unsigned v3, const void* sbase, unsigned lds) {
  lds = __builtin_amdgcn_readfirstlane(lds);
  if constexpr (POL == 1) asm volatile(DMA4(" nt") :: "s"(lds), "s"(sbase), "v"(v0), "v"(v1), "v"(v2), "v"(v3) : "memory");
  else if constexpr (POL == 2) asm volatile(DMA4(" sc1") :: "s"(lds), "s"(sbase), "v"(v0), "v"(v1), "v"(v2), "v"(v3) : "memory");
  else asm volatile(DMA4("") :: "s"(lds), "s"(sbase), "v"(v0), "v"(v1), "v"(v2), "v"(v3) : "memory");
}
DI void dma3(unsigned v0, unsigned v1, unsigned v2, const void* sbase, unsigned lds) {
  lds = __builtin_amdgcn_readfirstlane(lds);
  asm volatile(DMA_PRE "global_load_lds_dwordx4 %2, %1\n\tglobal_load_lds_dwordx4 %3, %1 offset:1024\n\tglobal_load_lds_dwordx4 %4, %1 offset:2048"
               :: "s"(lds), "s"(sbase), "v"(v0), "v"(v1), "v"(v2) : "memory");
}
DI void dma2nt(unsigned v0, const void* sbase, unsigned lds) {
  lds = __builtin_amdgcn_readfirstlane(lds);
  asm volatile(DMA_PRE "global_load_lds_dwordx4 %2, %1 nt\n\tglobal_load_lds_dwordx4 %2, %1 offset:1024 nt" :: "s"(lds), "s"(sbase), "v"(v0) : "memory");
}
template <int N> DI void waitvm() { asm volatile("s_waitcnt vmcnt(%0)" ::"i"(N) : "memory"); }
DI void bar() { asm volatile("s_waitcnt lgkmcnt(0)\n\ts_barrier" ::: "memory"); }
DI void waitlds() { asm volatile("s_waitcnt lgkmcnt(0)" ::: "memory"); }
DI unsigned pk2(float a, float b) { f32x2 t = {a, b}; bf16x2 p = __builtin_convertvector(t, bf16x2); return __builtin_bit_cast(unsigned, p); }
DI bf16x8 lds128(unsigned a) { return *(const LAS bf16x8*)(size_t)a; }
DI void sts128(unsigned a, u32x4 v) { *(LAS u32x4*)(size_t)a = v; }
DI bf16x8 ldstr(unsigned a0, unsigned a1) {
  bf16x4 a = __builtin_amdgcn_ds_read_tr16_b64_v4bf16((LAS bf16x4*)(size_t)a0);
  bf16x4 b = __builtin_amdgcn_ds_read_tr16_b64_v4bf16((LAS bf16x4*)(size_t)a1);
  return __builtin_shufflevector(a, b, 0, 1, 2, 3, 4, 5, 6, 7);
}
DI float xhalf_max(float v) { auto r = __builtin_amdgcn_permlane32_swap(__float_as_uint(v), __float_as_uint(v), false, false); return fmaxf(__uint_as_float(r[0]), __uint_as_float(r[1])); }
DI float xhalf_sum(float v) { auto r = __builtin_amdgcn_permlane32_swap(__float_as_uint(v), __float_as_uint(v), false, false); return __uint_as_float(r[0]) + __uint_as_float(r[1]); }
DI u32x4 pack8(const f32x16& a, int s) {
  u32x4 o = {pk2(a[8 * s], a[8 * s + 1]), pk2(a[8 * s + 2], a[8 * s + 3]), pk2(a[8 * s + 4], a[8 * s + 5]), pk2(a[8 * s + 6], a[8 * s + 7])};
  return o;
}
DI void pin(u32x4& x) { asm volatile("" : "+v"(x)); }
template <int NM, int ND> DI void ilv() {
#pragma unroll
  for (int i = 0; i < NM; ++i) { __builtin_amdgcn_sched_group_barrier(0x008, 1, 0); __builtin_amdgcn_sched_group_barrier(0x100, ND, 0); }
  __builtin_amdgcn_sched_barrier(0);
}
DI void st_sc1(__amdgpu_buffer_rsrc_t rs, unsigned off, u32x4 v) { __builtin_amdgcn_raw_buffer_store_b128(v, rs, off, 0, 16); }
DI u32x4 ld_sc1(__amdgpu_buffer_rsrc_t rs, unsigned off) { return __builtin_amdgcn_raw_buffer_load_b128(rs, off, 0, 16); }
#define RLX_AGENT __ATOMIC_RELAXED, __HIP_MEMORY_SCOPE_AGENT
constexpr unsigned SPIN_CAP = 1u << 18;

constexpr unsigned S(int i) { return 8 * KB * i; }
constexpr unsigned area(int a) { return a == 0 ? S(12) : a == 1 ? S(16) : a == 2 ? S(8) : a == 3 ? S(0) : S(4); }
constexpr unsigned AREA_TAB = 0x10243u;
constexpr unsigned QIMG = S(8), PB = S(8), PA = S(17), MISC = S(11);
constexpr unsigned L_MX = MISC, L_SM = MISC + 3072, L_TAG = MISC + 6144, L_CNT = MISC + 6400;
constexpr unsigned vslot(int j) { return j < 5 ? S(12 + j) : j < 13 ? S(j - 5) : S(12 + j - 13); }
constexpr unsigned oimg(int jj) { return jj == 0 ? S(12) : jj == 1 ? S(8) : jj == 2 ? S(0) : S(4); }
constexpr unsigned OIMG_TAB = 0x408Cu;
constexpr unsigned wobs(int jj) { return jj == 0 ? S(15) : S(16 + jj); }
constexpr unsigned wset(int set, int t) { return set == 0 ? (t == 0 ? S(16) : S(t - 1)) : S(3 + t); }
enum { C_O0 = 0, C_FREEX = 4, C_FREEY = 5, C_WOB = 6, C_REL0 = 7, C_L01 = 11, C_P01 = 12, C_L2 = 13, C_L3 = 14 };
DI void cnt_add(unsigned idx, unsigned n = 1) {
  unsigned long long keep; const unsigned a = L_CNT + idx * 4;
  asm volatile("s_mov_b64 %0, exec\n\ts_mov_b64 exec, 1\n\tds_add_u32 %1, %2\n\ts_mov_b64 exec, %0" : "=&s"(keep) : "v"(a), "v"(n) : "memory");
}
DI void cnt_wait(unsigned idx) {
  const unsigned a = L_CNT + idx * 4; unsigned v, s, n = 1u << 24;
  asm volatile("1:\n\tds_read_b32 %0, %3\n\ts_waitcnt lgkmcnt(0)\n\tv_readfirstlane_b32 %1, %0\n\ts_cmp_ge_u32 %1, 4\n\ts_cbranch_scc1 2f\n\t"
               "s_sleep 1\n\ts_sub_u32 %2, %2, 1\n\ts_cmp_lg_u32 %2, 0\n\ts_cbranch_scc1 1b\n2:"
               : "=&v"(v), "=&s"(s), "+s"(n) : "v"(a) : "memory", "scc");
}

__global__ __launch_bounds__(512, 2) void k_fused(const char* __restrict__ x, const char* __restrict__ kc, const char* __restrict__ vc,
                                                   const char* __restrict__ wq, const char* __restrict__ wo, char* __restrict__ ws, char* __restrict__ out) {
  const int g = blockIdx.x & 7, idx = blockIdx.x >> 3, b = idx >> 2, i4 = idx & 3, h = 4 * g + i4;
  const int c4 = (i4 + (b >> 1)) & 3;
  const bool tA = (i4 & 1) == 0;
  const int tid = threadIdx.x, lane = tid & 63;
  const int w = __builtin_amdgcn_readfirstlane(tid >> 6), dt = w & 3;
  const bool r1 = w >= 4;
  const int r = lane & 31, hh = lane >> 5;
  const int blk = (lane >> 4) & 1, q4 = (lane & 15) >> 2, p4 = lane & 3;
  const unsigned u = lane >> 4, l16 = lane * 16;
  STAMP(0);
  const char* kb = kc + ((size_t)(b * NH + h) * SK) * 256;
  const char* vb = vc + ((size_t)(b * NH + h) * SK) * 256;
  const unsigned vo_tr = u * 256 + (((lane & 15) ^ (u << 2)) * 16);
  const unsigned vqv = vo_tr + dt * 8192, vtv = vo_tr + dt * 2048;
  const unsigned k0v = u * 256 + (((lane & 15) ^ u) * 16) + dt * 8192, k1v = k0v ^ 64, k2v = k0v ^ 128, k3v = k0v ^ 192;
  auto karea = [&](int q) AI { return (q < 2 && tA) ? area(q) : area(q + 1); };
  auto issue_kq = [&](int q) AI {
    const unsigned d = karea(q) + dt * 8 * KB;
    dma4<1>(k0v, k1v, k2v, k3v, kb + q * 32768, d);
    dma4<1>(k0v, k1v, k2v, k3v, kb + q * 32768 + 4096, d + 4096);
  };
  auto issue_v = [&](int j) AI { dma2nt(vtv, vb + j * 8192, vslot(j) + dt * 2 * KB); };
  auto issue_vh = [&](int j0, int half) AI { dma4<1>(vqv, vqv, vqv, vqv, vb + j0 * 8192 + half * 4096, vslot(j0) + dt * 8 * KB + half * 4096); };
  bf16x8 kf[2][8];
  const unsigned kfo = dt * 8 * KB + r * 256 + ((hh ^ (r & 15)) * 16);
  auto pull_k = [&](int kt) AI {
    const unsigned slot = (r1 ? area(3 + kt) : karea(kt)) + kfo;
#pragma unroll
    for (int s = 0; s < 8; ++s) kf[kt][s] = lds128(slot ^ (s * 32));
  };

  if (!r1) {
    const unsigned epoch = __hip_atomic_load((GAS unsigned*)(ws + WS_EPOCH + (b * 8 + g) * 64), RLX_AGENT);
    {
      unsigned long long pc; unsigned len;
      asm volatile("s_getpc_b64 %0\n.Lk1_here:\n\ts_mov_b32 %1, .Lk2_end-.Lk1_here" : "=s"(pc), "=s"(len));
      const unsigned off = (idx * 4 + dt) * 512 + (lane & 7) * 64;
      unsigned sink = 0;
      if (lane < 8 && off < len) sink = *(const GAS unsigned*)(pc + off);
      if (0 && lane >= 8 && lane < 8 + 12) sink = *(const GAS unsigned*)(ws + (size_t)(lane - 8) * (2u << 20) + (1u << 20) - 4096 + idx * 64);
      asm volatile("" :: "v"(sink));
    }
    if (!tA) { bar(); pull_k(0); bar(); pull_k(1); }
    STAMP(1);
    f32x16 qacc[3] = {};
    const int p4s = ((p4 & 1) << 1) | (p4 >> 1);
    const unsigned a_off = (8 * hh + q4) * 256 + (((4 * dt + 2 * blk + (p4s >> 1)) ^ (q4 << 2)) * 16) + 8 * (p4s & 1);
    const unsigned x_off = r * 128 + 16 * KB + ((hh ^ ((r >> 1) & 7)) * 16);
    bf16x8 fa[4] = {}, fb[4][3] = {};
    auto read_q = [&](unsigned base, int ks) AI {
      fa[ks] = ldstr(base + a_off + ks * 4096, base + a_off + ks * 4096 + 1024);
#pragma unroll
      for (int j = 0; j < 3; ++j) fb[ks][j] = lds128(base + (x_off ^ (ks * 32)) + j * 4096);
    };
    auto mma_q = [&](int ks) AI {
#pragma unroll
      for (int j = 0; j < 3; ++j) qacc[j] = MFMA(fa[ks], fb[ks][j], qacc[j]);
    };
    NOUNROLL
    for (int it = 0; it < 2; ++it) {
#pragma unroll
      for (int a = 0; a < 5; ++a) {
        constexpr unsigned dummy = 0; (void)dummy;
        const unsigned base = area(a);
        bar(); SB();
        read_q(base, 0); read_q(base, 1); mma_q(2); ilv<3, 4>();
        read_q(base, 2); mma_q(3); ilv<3, 2>();
        read_q(base, 3); mma_q(0); ilv<3, 2>();
        mma_q(1); SB();
      }
    }
    mma_q(2); mma_q(3);
#pragma unroll
    for (int j = 0; j < 3; ++j)
#pragma unroll
      for (int sx = 0; sx < 2; ++sx) sts128(QIMG + (j * 8 + sx) * KB + dt * 2 * KB + l16, pack8(qacc[j], sx));
    if (tid < 16) *(LAS unsigned*)(size_t)(L_CNT + tid * 4) = 0u;
    if (tid == 0) *(LAS unsigned*)(size_t)L_TAG = epoch * 1664525u + 1013904223u;
    bar();
    STAMP(2);
  } else {
    const char* xb = x + (size_t)b * SQ * DM * 2;
    const char* wqh = wq + (size_t)h * 256;
    const unsigned vo_x = (lane >> 3) * 1280 + (((lane & 7) ^ u) * 16);
    const unsigned wq0 = u * 8192 + (((lane & 15) ^ (u << 2)) * 16) + dt * 16 * 8192, wq1 = wq0 + (32768 - 1024), wq2 = wq0 + 2 * (32768 - 1024), wq3 = wq0 + 3 * (32768 - 1024);
    const unsigned xb0 = (vo_x ^ ((dt & 1) << 6)) + dt * 3 * 10240, xb1 = (vo_x ^ (((dt + 1) & 1) << 6)) + dt * 3 * 10240 + (10240 - 1024), xb2 = xb0 + 2 * (10240 - 1024);
    const int rot = (b + 3 * i4 + g) % 10;
    auto issue_q = [&](int s0) AI {
      int s = s0 + rot; if (s >= 10) s -= 10;
      const unsigned base = ((AREA_TAB >> (4 * (s0 >= 5 ? s0 - 5 : s0))) & 15u) << 15;
      dma4<0>(wq0, wq1, wq2, wq3, wqh + (size_t)s * (64 * 8192), base + 4 * dt * KB);
      dma3(xb0, xb1, xb2, xb + s * 128, base + 16 * KB + 3 * dt * KB);
    };
    if (tA) {
      issue_q(0); issue_q(1); issue_q(2); waitvm<14>(); bar();
    } else {
      issue_kq(0); issue_kq(1); issue_kq(2);
      waitvm<16>(); bar(); issue_kq(3);
      waitvm<16>(); bar(); issue_q(0);
      waitvm<15>(); pull_k(0); issue_q(1);
      waitvm<14>(); pull_k(1);
      STAMP(1);
      waitvm<7>(); bar(); issue_q(2);
    }
    NOUNROLL
    for (int s0 = 0; s0 < 7; ++s0) { issue_q(s0 + 3); waitvm<14>(); bar(); }
    if (tA) {
      issue_kq(0); waitvm<15>(); bar();
      issue_kq(1); waitvm<16>(); bar();
      issue_kq(2); waitvm<16>(); bar();
      issue_kq(3); waitvm<16>(); bar();
    } else {
      waitvm<7>(); bar();
      issue_vh(0, 0); issue_vh(0, 1); waitvm<8>(); bar();
      issue_v(4); bar();
      issue_vh(5, 0); issue_vh(5, 1);
    }
    STAMP(2);
    bar();
  }

  f32x16 sacc[2][3] = {};
  {
    bf16x8 qf[2][6];
    auto rd_q6 = [&](int st) AI {
#pragma unroll
      for (int i = 0; i < 6; ++i) qf[st & 1][i] = lds128(QIMG + ((i % 3) * 8 + 2 * st + i / 3) * KB + l16);
    };
    auto s_tile = [&](int kt) AI {
      rd_q6(0); SB();
#pragma unroll
      for (int st = 0; st < 4; ++st) {
        if (st < 3) rd_q6(st + 1);
#pragma unroll
        for (int i = 0; i < 6; ++i) sacc[kt][i % 3] = MFMA(kf[kt][2 * st + i / 3], qf[st & 1][i], sacc[kt][i % 3]);
        ilv<6, 1>();
      }
    };
    if (tA) {
      if (r1) waitvm<8>(); pull_k(0); if (r1) issue_vh(0, 0);
      s_tile(0);
      if (r1) { issue_vh(0, 1); waitvm<8>(); } else bar(); pull_k(1); if (r1) issue_v(4);
      s_tile(1);
    } else {
      rd_q6(0); SB();
#pragma unroll
      for (int st = 0; st < 4; ++st) {
        if (st < 3) rd_q6(st + 1);
#pragma unroll
        for (int i = 0; i < 6; ++i) {
          sacc[0][i % 3] = MFMA(kf[0][2 * st + i / 3], qf[st & 1][i], sacc[0][i % 3]);
          sacc[1][i % 3] = MFMA(kf[1][2 * st + i / 3], qf[st & 1][i], sacc[1][i % 3]);
        }
        ilv<6, 1>();
      }
    }
  }
  const unsigned mxo = r * 32;
#pragma unroll
  for (int qb = 0; qb < 3; ++qb) {
    float m = sacc[0][qb][0];
#pragma unroll
    for (int kt = 0; kt < 2; ++kt)
#pragma unroll
      for (int i = 0; i < 16; ++i) m = fmaxf(m, sacc[kt][qb][i]);
    m = xhalf_max(m);
    if (hh == 0) *(LAS float*)(size_t)(L_MX + qb * 1024 + mxo + w * 4) = m;
  }
  if (r1) {
    if (tA) { issue_vh(5, 0); issue_vh(5, 1); waitvm<10>(); } else { issue_vh(9, 0); issue_vh(9, 1); waitvm<18>(); }
  }
  bar();
  STAMP(3);
  const float cs = 0.08838834764831845f * 1.4426950408889634f;
  auto max4 = [&](int qb, int half) AI {
    f32x4 m = *(const LAS f32x4*)(size_t)(L_MX + qb * 1024 + mxo + half * 16);
    return fmaxf(fmaxf(m[0], m[1]), fmaxf(m[2], m[3]));
  };
  float m0[3], lsum[3] = {0.f, 0.f, 0.f};
#pragma unroll
  for (int qb = 0; qb < 3; ++qb) m0[qb] = fmaxf(max4(qb, 0), max4(qb, r1));
  u32x4 pr[3][2];
  auto exp_tile = [&](int kt) AI {
#pragma unroll
    for (int qb = 0; qb < 3; ++qb) {
      const float nm = -m0[qb] * cs;
#pragma unroll
      for (int i = 0; i < 18; ++i) {
        if (i < 16) sacc[kt][qb][i] = __builtin_amdgcn_exp2f(fmaf(sacc[kt][qb][i], cs, nm));
        if (i >= 2) asm volatile("v_add_f32 %0, %0, %1" : "+v"(lsum[qb]) : "v"(sacc[kt][qb][i - 2]));
        if (i >= 2 && (i & 1)) pr[qb][(i - 2) >> 3][((i - 2) >> 1) & 3] = pk2(sacc[kt][qb][i - 3], sacc[kt][qb][i - 2]);
        SB();
      }
      pin(pr[qb][0]); pin(pr[qb][1]);
    }
  };
  auto put_p = [&](int kt) AI {
#pragma unroll
    for (int qb = 0; qb < 3; ++qb)
#pragma unroll
      for (int sx = 0; sx < 2; ++sx) sts128((kt == 0 ? PA : PB) + dt * 6 * KB + l16 + (sx * 3 + qb) * KB, pr[qb][sx]);
  };
  const char* wob = wo + (size_t)(4 * g) * 128 * 1280 + c4 * 320;
  const unsigned wa0 = u * 1280 + (((lane & 15) ^ (u << 2)) * 16) + dt * 32 * 1280, wa1 = wa0 + (5120 - 1024), wa2 = wa0 + 2 * (5120 - 1024), wa3 = wa0 + 3 * (5120 - 1024);
  auto issue_wa = [&](int jj) AI {
    const char* src = wob + (size_t)(((i4 + jj) & 3) * 128) * 1280;
    const unsigned d = (jj & 1) ? S(3) + dt * 8 * KB : (dt == 0 ? S(16) : (dt - 1) * 8 * KB);
    dma4<0>(wa0, wa1, wa2, wa3, src, d);
    dma4<0>(wa0, wa1, wa2, wa3, src + 16 * 1280, d + 4096);
  };
  if (r1 && tA) issue_vh(9, 0);
  exp_tile(0);
  if (r1) {
    if (tA) issue_vh(9, 1);
    cnt_wait(C_REL0); issue_v(13); issue_v(14); issue_v(15);
    waitvm<14>();
    bar();
  }
  put_p(0);
  exp_tile(1);
#pragma unroll
  for (int qb = 0; qb < 3; ++qb) {
    const float s = xhalf_sum(lsum[qb]);
    if (hh == 0) *(LAS float*)(size_t)(L_SM + qb * 1024 + mxo + w * 4) = s;
  }
  if (r1) { cnt_wait(C_REL0 + 1); issue_wa(0); waitvm<14>(); }
  STAMP(4);
  bar();
  STAMP(5);
  put_p(1);

  const unsigned v_off = (4 * hh + q4) * 256 + (((4 * dt + 2 * blk + (p4 >> 1)) ^ (q4 << 2)) * 16) + 8 * (p4 & 1);
  const unsigned tag = __builtin_amdgcn_readfirstlane(*(const LAS unsigned*)(size_t)L_TAG);
  f32x16 acc[3] = {};
  if (!r1) {
    f32x16 oa[2][3] = {};
    bf16x8 vf[4][2], pf[2][2][3];
    auto rd_v = [&](int j) AI {
#pragma unroll
      for (int sx = 0; sx < 2; ++sx) vf[j & 3][sx] = ldstr(vslot(j) + v_off + sx * 4096, vslot(j) + v_off + sx * 4096 + 2048);
    };
    auto rd_p = [&](int j) AI {
#pragma unroll
      for (int sx = 0; sx < 2; ++sx)
#pragma unroll
        for (int qb = 0; qb < 3; ++qb) pf[j & 1][sx][qb] = lds128((((j >> 2) & 1) ? PB : PA) + ((j & 3) * 6 + sx * 3 + qb) * KB + l16);
    };
    auto mma_pv = [&](int j) AI {
#pragma unroll
      for (int sx = 0; sx < 2; ++sx)
#pragma unroll
        for (int qb = 0; qb < 3; ++qb) oa[j >> 3][qb] = MFMA(vf[j & 3][sx], pf[j & 1][sx][qb], oa[j >> 3][qb]);
    };
    auto pv_quarter = [&](int q) AI {
      rd_v(4 * q); rd_p(4 * q); rd_v(4 * q + 1); rd_v(4 * q + 2);
      if (q > 0) { mma_pv(4 * q - 1); ilv<6, 3>(); } else SB();
      rd_v(4 * q + 3); rd_p(4 * q + 1); mma_pv(4 * q); ilv<6, 2>();
      rd_p(4 * q + 2); mma_pv(4 * q + 1); ilv<6, 1>();
      asm volatile("s_waitcnt lgkmcnt(6)" ::: "memory"); cnt_add(C_REL0 + q); SB();
      rd_p(4 * q + 3); mma_pv(4 * q + 2); ilv<6, 1>();
    };
    pv_quarter(0); bar(); SB();
    pv_quarter(1); bar(); SB();
    STAMP(6);
    pv_quarter(2);
    float sc0[3], sc1[3];
#pragma unroll
    for (int qb = 0; qb < 3; ++qb) {
      const float m = fmaxf(m0[qb], max4(qb, 1));
      const float al = __builtin_amdgcn_exp2f((m0[qb] - m) * cs);
      f32x4 l0 = *(const LAS f32x4*)(size_t)(L_SM + qb * 1024 + mxo);
      f32x4 l1 = *(const LAS f32x4*)(size_t)(L_SM + qb * 1024 + mxo + 16);
      const float l = al * ((l0[0] + l0[1]) + (l0[2] + l0[3])) + ((l1[0] + l1[1]) + (l1[2] + l1[3]));
      sc1[qb] = 1.0f / l; sc0[qb] = al * sc1[qb];
    }
    bar(); SB();
    STAMP(7);
    pv_quarter(3);
#pragma unroll
    for (int qb = 0; qb < 3; ++qb)
#pragma unroll
      for (int i = 0; i < 16; ++i) oa[0][qb][i] *= sc0[qb];
    mma_pv(15);
    const char* obase = ws + WS_O + (size_t)(b * NH + 4 * g) * 24576;
    const __amdgpu_buffer_rsrc_t ors = __builtin_amdgcn_make_buffer_rsrc((char*)obase, 0, 4 * 24576, 0x00020000);
    cnt_wait(C_REL0 + 3);
#pragma unroll
    for (int qb = 0; qb < 3; ++qb) {
#pragma unroll
      for (int i = 0; i < 16; ++i) oa[1][qb][i] = fmaf(oa[1][qb][i], sc1[qb], oa[0][qb][i]);
#pragma unroll
      for (int sx = 0; sx < 2; ++sx) {
        const u32x4 o = pack8(oa[1][qb], sx);
        st_sc1(ors, i4 * 24576 + (qb * 8 + sx) * KB + dt * 2 * KB + l16, o);
        sts128(oimg(0) + (qb * 8 + sx) * KB + dt * 2 * KB + l16, o);
      }
    }
    waitlds(); cnt_add(C_O0);
    STAMP(8);
    waitvm<0>();
    GAS unsigned* fl = (GAS unsigned*)(ws + WS_F1 + (size_t)(b * 8 + g) * 64);
    if (lane == 0) __hip_atomic_store(fl + dt * 4 + i4, tag, RLX_AGENT);
    STAMP(9);
    if (w == 3) {
      unsigned pend = 0xEu;
      cnt_wait(C_O0);
      NOUNROLL
      for (unsigned spin = 0; pend && spin < SPIN_CAP; ++spin) {
        const unsigned fv = __hip_atomic_load(fl + (lane & 12) + ((i4 + lane) & 3), RLX_AGENT);
        const unsigned m = (unsigned)__ballot(fv == tag);
        const unsigned rdy = m & (m >> 4) & (m >> 8) & (m >> 12) & pend;
        if (!rdy) { __builtin_amdgcn_s_sleep(1); continue; }
        NOUNROLL
        for (unsigned t = rdy; t; t &= t - 1) {
          const unsigned jj = __builtin_ctz(t);
          if (jj >= 2) cnt_wait(C_FREEX + jj - 2);
          const char* src = obase + ((i4 + jj) & 3) * 24576;
          const unsigned d = ((OIMG_TAB >> (4 * jj)) & 15u) << 13;
          NOUNROLL
          for (int i = 0; i < 6; ++i) dma4<2>(l16, l16, l16, l16, src + i * 4096, d + i * 4096);
        }
        waitvm<0>();
        NOUNROLL
        for (unsigned t = rdy; t; t &= t - 1) cnt_add(__builtin_ctz(t), 4);
        pend &= ~rdy;
      }
      if (i4 == 0 && lane == 0) __hip_atomic_store((GAS unsigned*)(ws + WS_EPOCH + (b * 8 + g) * 64), tag, RLX_AGENT);
    } else {
      const unsigned w_off = (4 * hh + q4) * 64 + (2 * blk + (p4 >> 1)) * 16 + 8 * (p4 & 1);
      cnt_wait(C_WOB);
      NOUNROLL
      for (int jj = 0; jj < 4; ++jj) {
        cnt_wait(jj);
        const unsigned wb = (jj == 0 ? S(15) : S(16) + jj * 8 * KB) + w_off;
        const unsigned ob = (((OIMG_TAB >> (4 * jj)) & 15u) << 13) + dt * 8 * KB + l16;
        bf16x8 af[8], of[8];
#pragma unroll
        for (int ds = 0; ds < 8; ++ds) { af[ds] = ldstr(wb + ds * KB, wb + ds * KB + 512); of[ds] = lds128(ob + ds * KB); }
#pragma unroll
        for (int ds = 0; ds < 8; ++ds) acc[0] = MFMA(af[ds], of[ds], acc[0]);
      }
    }
  } else {
    bf16x8 wf[4][8];
    auto pull_wa = [&](int jj) AI {
#pragma unroll
      for (int ds = 0; ds < 8; ++ds) { const unsigned a = wset(jj & 1, ds >> 1) + v_off + (ds & 1) * 4096; wf[jj][ds] = ldstr(a, a + 2048); }
    };
    auto mma_o = [&](int jj) AI {
      bf16x8 of[3][3];
#pragma unroll
      for (int ds = 0; ds < 2; ++ds)
#pragma unroll
        for (int qb = 0; qb < 3; ++qb) of[ds][qb] = lds128(oimg(jj) + (qb * 8 + ds) * KB + l16);
      SB();
#pragma unroll
      for (int ds = 0; ds < 8; ++ds) {
        if (ds < 6) {
#pragma unroll
          for (int qb = 0; qb < 3; ++qb) of[(ds + 2) % 3][qb] = lds128(oimg(jj) + (qb * 8 + ds + 2) * KB + l16);
        }
#pragma unroll
        for (int qb = 0; qb < 3; ++qb) acc[qb] = MFMA(wf[jj][ds], of[ds % 3][qb], acc[qb]);
        ilv<3, 1>();
      }
    };
    cnt_wait(C_REL0 + 2); issue_wa(1);
    waitvm<16>();
    bar();
    STAMP(6);
    {
      const unsigned wb0 = (lane >> 2) * 1280 + (lane & 3) * 16 + 256, wb1 = wb0 + (20480 - 1024), wb2 = wb0 + 2 * (20480 - 1024), wb3 = wb0 + 3 * (20480 - 1024);
      const char* src = wob + (size_t)(((i4 + dt) & 3) * 128) * 1280;
      const unsigned d = dt == 0 ? S(15) : S(16) + dt * 8 * KB;
      dma4<0>(wb0, wb1, wb2, wb3, src, d);
      dma4<0>(wb0, wb1, wb2, wb3, src + 64 * 1280, d + 4096);
    }
    waitvm<8>(); cnt_add(C_L01); cnt_wait(C_L01);
    pull_wa(0); pull_wa(1); waitlds(); cnt_add(C_P01); cnt_wait(C_P01);
    STAMP(7);
    issue_wa(2); issue_wa(3);
    waitvm<16>(); cnt_add(C_WOB);
    waitvm<8>(); cnt_add(C_L2); cnt_wait(C_L2); pull_wa(2); waitlds(); cnt_add(C_FREEX);
    waitvm<0>(); cnt_add(C_L3); cnt_wait(C_L3); pull_wa(3); waitlds(); cnt_add(C_FREEY);
    STAMP(8);
    cnt_wait(0); mma_o(0);
    STAMP(9);
    cnt_wait(1); mma_o(1);
    STAMP(10);
    cnt_wait(2); mma_o(2);
    STAMP(11);
    cnt_wait(3); mma_o(3);
  }
  STAMP(12);
  if (w != 3) {
    const __amdgpu_buffer_rsrc_t prs = __builtin_amdgcn_make_buffer_rsrc(ws + WS_P + (size_t)(b * 4 + c4) * PBYTES, 0, PBYTES, 0x00020000);
    const unsigned so = (((r >> 2) * 8 + w) * 6) * KB + (g * 8 + (r & 3) + 4 * hh) * 16;
#pragma unroll
    for (int t = 0; t < 3; ++t) if (t == 0 || r1) {
#pragma unroll
      for (int uu = 0; uu < 2; ++uu) st_sc1(prs, so + (t * 2 + uu) * KB, pack8(acc[t], uu));
    }
  }
}

__global__ __launch_bounds__(256) void k_reduce(const char* __restrict__ ws, char* __restrict__ out) {
  const int lane = threadIdx.x & 63;
  const int item = blockIdx.x * 4 + __builtin_amdgcn_readfirstlane(threadIdx.x >> 6);
  const int grp = item / 15, tile = item - grp * 15;
  const int g = grp & 7, bc = grp >> 3, b = bc >> 2, c4 = bc & 3;
  const int nt = tile / 3, qb = tile - nt * 3;
  const int w = nt < 4 ? 4 + nt : qb, tt = nt < 4 ? qb : 0;
  const char* pb = ws + WS_P + (size_t)bc * PBYTES + ((g * 8 + w) * 6 + tt * 2) * KB + lane * 16;
  const u32x4 t0 = *(const u32x4*)pb, t1 = *(const u32x4*)(pb + KB);
  float xx[16], y[8], z[4];
#pragma unroll
  for (int i = 0; i < 8; ++i) { const unsigned d = i < 4 ? t0[i & 3] : t1[i & 3]; xx[2 * i] = __uint_as_float(d << 16); xx[2 * i + 1] = __uint_as_float(d & 0xffff0000u); }
#pragma unroll
  for (int i = 0; i < 8; ++i) { auto q = __builtin_amdgcn_permlane32_swap(__float_as_uint(xx[i]), __float_as_uint(xx[i + 8]), false, false); y[i] = __uint_as_float(q[0]) + __uint_as_float(q[1]); }
#pragma unroll
  for (int i = 0; i < 4; ++i) { auto q = __builtin_amdgcn_permlane16_swap(__float_as_uint(y[i]), __float_as_uint(y[i + 4]), false, false); z[i] = __uint_as_float(q[0]) + __uint_as_float(q[1]); }
#pragma unroll
  for (int i = 0; i < 4; ++i) z[i] += __uint_as_float(__builtin_amdgcn_update_dpp(0u, __float_as_uint(z[i]), 0x128, 0xf, 0xf, false));
  const u32x2 o = {pk2(z[0], z[1]), pk2(z[2], z[3])};
  if ((lane & 8) == 0) *(u32x2*)(out + ((size_t)(b * SQ + 32 * qb + 4 * g + (lane & 3)) * DM + 160 * c4 + 32 * nt + 8 * (lane >> 4) + (lane & 4)) * 2) = o;
  asm volatile(".Lk2_end:");
}

extern "C" void kernel_launch(void* const* d_in, const int* in_sizes, int n_in, void* d_out, int out_size, void* d_ws, size_t ws_size,
                              hipStream_t stream) {
  (void)hipFuncSetAttribute((const void*)k_fused, hipFuncAttributeMaxDynamicSharedMemorySize, LDS_BYTES);
  hipLaunchKernelGGL(k_fused, dim3(256), dim3(512), LDS_BYTES, stream, (const char*)d_in[0], (const char*)d_in[1], (const char*)d_in[2],
                     (const char*)d_in[3], (const char*)d_in[4], (char*)d_ws, (char*)d_out);
  hipLaunchKernelGGL(k_reduce, dim3(960), dim3(256), 0, stream, (const char*)d_ws, (char*)d_out);
}
```

```cpp
#include <hip/hip_runtime.h>
#include <hip/hip_bf16.h>

typedef __attribute__((ext_vector_type(8))) __bf16 bf16x8;
typedef __attribute__((ext_vector_type(4))) __bf16 bf16x4;
typedef __attribute__((ext_vector_type(2))) __bf16 bf16x2;
typedef __attribute__((ext_vector_type(16))) float f32x16;
typedef __attribute__((ext_vector_type(4))) float f32x4;
typedef __attribute__((ext_vector_type(2))) float f32x2;
typedef __attribute__((ext_vector_type(4))) unsigned u32x4;
typedef __attribute__((ext_vector_type(2))) unsigned u32x2;
#define LAS __attribute__((address_space(3)))
#define GAS __attribute__((address_space(1)))
#define DI __device__ __forceinline__
#define AI __attribute__((always_inline))
#define MFMA(a, b, c) __builtin_amdgcn_mfma_f32_32x32x16_bf16((a), (b), (c), 0, 0, 0)
#define SB() __builtin_amdgcn_sched_barrier(0)
#define NOUNROLL _Pragma("clang loop unroll(disable)")

#define STAMP(i) do {} while (0)

constexpr int SQ = 96, DM = 640, NH = 32, SK = 512;
constexpr unsigned KB = 1024;
constexpr unsigned LDS_BYTES = 163840;

constexpr size_t WS_EPOCH = 0;
constexpr size_t WS_F1 = 4096;
constexpr size_t WS_F2 = 16384;
constexpr size_t WS_O = 1u << 20;
constexpr size_t WS_P = 8u << 20;
constexpr unsigned PBYTES = 8 * 15 * 2 * 1024;

#define DMA_PRE "s_mov_b32 m0, %0\n\ts_nop 0\n\t"
#define DMA4(SUF) DMA_PRE "global_load_lds_dwordx4 %2, %1" SUF "\n\tglobal_load_lds_dwordx4 %3, %1 offset:1024" SUF \
                  "\n\tglobal_load_lds_dwordx4 %4, %1 offset:2048" SUF "\n\tglobal_load_lds_dwordx4 %5, %1 offset:3072" SUF
template <int POL> DI void dma4(unsigned v0, unsigned v1, unsigned v2, unsigned v3, const void* sbase, unsigned lds) {
  lds = __builtin_amdgcn_readfirstlane(lds);
  if constexpr (POL == 1) asm volatile(DMA4(" nt") :: "s"(lds), "s"(sbase), "v"(v0), "v"(v1), "v"(v2), "v"(v3) : "memory");
  else if constexpr (POL == 2) asm volatile(DMA4(" sc1") :: "s"(lds), "s"(sbase), "v"(v0), "v"(v1), "v"(v2), "v"(v3) : "memory");
  else asm volatile(DMA4("") :: "s"(lds), "s"(sbase), "v"(v0), "v"(v1), "v"(v2), "v"(v3) : "memory");
}
DI void dma3(unsigned v0, unsigned v1, unsigned v2, const void* sbase, unsigned lds) {
  lds = __builtin_amdgcn_readfirstlane(lds);
  asm volatile(DMA_PRE "global_load_lds_dwordx4 %2, %1\n\tglobal_load_lds_dwordx4 %3, %1 offset:1024\n\tglobal_load_lds_dwordx4 %4, %1 offset:2048"
               :: "s"(lds), "s"(sbase), "v"(v0), "v"(v1), "v"(v2) : "memory");
}
DI void dma2nt(unsigned v0, const void* sbase, unsigned lds) {
  lds = __builtin_amdgcn_readfirstlane(lds);
  asm volatile(DMA_PRE "global_load_lds_dwordx4 %2, %1 nt\n\tglobal_load_lds_dwordx4 %2, %1 offset:1024 nt" :: "s"(lds), "s"(sbase), "v"(v0) : "memory");
}
template <int N> DI void waitvm() { asm volatile("s_waitcnt vmcnt(%0)" ::"i"(N) : "memory"); }
DI void bar() { asm volatile("s_waitcnt lgkmcnt(0)\n\ts_barrier" ::: "memory"); }
DI void waitlds() { asm volatile("s_waitcnt lgkmcnt(0)" ::: "memory"); }
DI unsigned pk2(float a, float b) { f32x2 t = {a, b}; bf16x2 p = __builtin_convertvector(t, bf16x2); return __builtin_bit_cast(unsigned, p); }
DI bf16x8 lds128(unsigned a) { return *(const LAS bf16x8*)(size_t)a; }
DI void sts128(unsigned a, u32x4 v) { *(LAS u32x4*)(size_t)a = v; }
DI bf16x8 ldstr(unsigned a0, unsigned a1) {
  bf16x4 a = __builtin_amdgcn_ds_read_tr16_b64_v4bf16((LAS bf16x4*)(size_t)a0);
  bf16x4 b = __builtin_amdgcn_ds_read_tr16_b64_v4bf16((LAS bf16x4*)(size_t)a1);
  return __builtin_shufflevector(a, b, 0, 1, 2, 3, 4, 5, 6, 7);
}
DI float xhalf_max(float v) { auto r = __builtin_amdgcn_permlane32_swap(__float_as_uint(v), __float_as_uint(v), false, false); return fmaxf(__uint_as_float(r[0]), __uint_as_float(r[1])); }
DI float xhalf_sum(float v) { auto r = __builtin_amdgcn_permlane32_swap(__float_as_uint(v), __float_as_uint(v), false, false); return __uint_as_float(r[0]) + __uint_as_float(r[1]); }
DI u32x4 pack8(const f32x16& a, int s) {
  u32x4 o = {pk2(a[8 * s], a[8 * s + 1]), pk2(a[8 * s + 2], a[8 * s + 3]), pk2(a[8 * s + 4], a[8 * s + 5]), pk2(a[8 * s + 6], a[8 * s + 7])};
  return o;
}
DI void pin(u32x4& x) { asm volatile("" : "+v"(x)); }
template <int NM, int ND> DI void ilv() {
#pragma unroll
  for (int i = 0; i < NM; ++i) { __builtin_amdgcn_sched_group_barrier(0x008, 1, 0); __builtin_amdgcn_sched_group_barrier(0x100, ND, 0); }
  __builtin_amdgcn_sched_barrier(0);
}
DI void st_sc1(__amdgpu_buffer_rsrc_t rs, unsigned off, u32x4 v) { __builtin_amdgcn_raw_buffer_store_b128(v, rs, off, 0, 16); }
DI u32x4 ld_sc1(__amdgpu_buffer_rsrc_t rs, unsigned off) { return __builtin_amdgcn_raw_buffer_load_b128(rs, off, 0, 16); }
#define RLX_AGENT __ATOMIC_RELAXED, __HIP_MEMORY_SCOPE_AGENT
constexpr unsigned SPIN_CAP = 1u << 18;

constexpr unsigned S(int i) { return 8 * KB * i; }
constexpr unsigned area(int a) { return a == 0 ? S(12) : a == 1 ? S(16) : a == 2 ? S(8) : a == 3 ? S(0) : S(4); }
constexpr unsigned AREA_TAB = 0x10243u;
constexpr unsigned QIMG = S(8), PB = S(8), PA = S(17), MISC = S(11);
constexpr unsigned L_MX = MISC, L_SM = MISC + 3072, L_TAG = MISC + 6144, L_CNT = MISC + 6400;
constexpr unsigned vslot(int j) { return j < 5 ? S(12 + j) : j < 13 ? S(j - 5) : S(12 + j - 13); }
constexpr unsigned oimg(int jj) { return jj == 0 ? S(12) : jj == 1 ? S(8) : jj == 2 ? S(0) : S(4); }
constexpr unsigned OIMG_TAB = 0x408Cu;
constexpr unsigned wobs(int jj) { return jj == 0 ? S(15) : S(16 + jj); }
constexpr unsigned wset(int set, int t) { return set == 0 ? (t == 0 ? S(16) : S(t - 1)) : S(3 + t); }
enum { C_O0 = 0, C_FREEX = 4, C_FREEY = 5, C_WOB = 6, C_REL0 = 7, C_L01 = 11, C_P01 = 12, C_L2 = 13, C_L3 = 14 };
DI void cnt_add(unsigned idx, unsigned n = 1) {
  unsigned long long keep; const unsigned a = L_CNT + idx * 4;
  asm volatile("s_mov_b64 %0, exec\n\ts_mov_b64 exec, 1\n\tds_add_u32 %1, %2\n\ts_mov_b64 exec, %0" : "=&s"(keep) : "v"(a), "v"(n) : "memory");
}
DI void cnt_wait(unsigned idx) {
  const unsigned a = L_CNT + idx * 4; unsigned v, s, n = 1u << 24;
  asm volatile("1:\n\tds_read_b32 %0, %3\n\ts_waitcnt lgkmcnt(0)\n\tv_readfirstlane_b32 %1, %0\n\ts_cmp_ge_u32 %1, 4\n\ts_cbranch_scc1 2f\n\t"
               "s_sleep 1\n\ts_sub_u32 %2, %2, 1\n\ts_cmp_lg_u32 %2, 0\n\ts_cbranch_scc1 1b\n2:"
               : "=&v"(v), "=&s"(s), "+s"(n) : "v"(a) : "memory", "scc");
}

__global__ __launch_bounds__(512, 2) void k_fused(const char* __restrict__ x, const char* __restrict__ kc, const char* __restrict__ vc,
                                                   const char* __restrict__ wq, const char* __restrict__ wo, char* __restrict__ ws, char* __restrict__ out) {
  const int g = blockIdx.x & 7, idx = blockIdx.x >> 3, b = idx >> 2, i4 = idx & 3, h = 4 * g + i4;
  const int c4 = (i4 + (b >> 1)) & 3;
  const bool tA = (i4 & 1) == 0;
  const int tid = threadIdx.x, lane = tid & 63;
  const int w = __builtin_amdgcn_readfirstlane(tid >> 6), dt = w & 3;
  const bool r1 = w >= 4;
  const int r = lane & 31, hh = lane >> 5;
  const int blk = (lane >> 4) & 1, q4 = (lane & 15) >> 2, p4 = lane & 3;
  const unsigned u = lane >> 4, l16 = lane * 16;
  STAMP(0);
  const char* kb = kc + ((size_t)(b * NH + h) * SK) * 256;
  const char* vb = vc + ((size_t)(b * NH + h) * SK) * 256;
  const unsigned vo_tr = u * 256 + (((lane & 15) ^ (u << 2)) * 16);
  const unsigned vqv = vo_tr + dt * 8192, vtv = vo_tr + dt * 2048;
  const unsigned k0v = u * 256 + (((lane & 15) ^ u) * 16) + dt * 8192, k1v = k0v ^ 64, k2v = k0v ^ 128, k3v = k0v ^ 192;
  auto karea = [&](int q) AI { return (q < 2 && tA) ? area(q) : area(q + 1); };
  auto issue_kq = [&](int q) AI {
    const unsigned d = karea(q) + dt * 8 * KB;
    dma4<1>(k0v, k1v, k2v, k3v, kb + q * 32768, d);
    dma4<1>(k0v, k1v, k2v, k3v, kb + q * 32768 + 4096, d + 4096);
  };
  auto issue_v = [&](int j) AI { dma2nt(vtv, vb + j * 8192, vslot(j) + dt * 2 * KB); };
  auto issue_vh = [&](int j0, int half) AI { dma4<1>(vqv, vqv, vqv, vqv, vb + j0 * 8192 + half * 4096, vslot(j0) + dt * 8 * KB + half * 4096); };
  bf16x8 kf[2][8];
  const unsigned kfo = dt * 8 * KB + r * 256 + ((hh ^ (r & 15)) * 16);
  auto pull_k = [&](int kt) AI {
    const unsigned slot = (r1 ? area(3 + kt) : karea(kt)) + kfo;
#pragma unroll
    for (int s = 0; s < 8; ++s) kf[kt][s] = lds128(slot ^ (s * 32));
  };

  if (!r1) {
    const unsigned epoch = __hip_atomic_load((GAS unsigned*)(ws + WS_EPOCH + (b * 8 + g) * 64), RLX_AGENT);
    {
      unsigned long long pc; unsigned len;
      asm volatile("s_getpc_b64 %0\n.Lk1_here:\n\ts_mov_b32 %1, .Lk2_end-.Lk1_here" : "=s"(pc), "=s"(len));
      const unsigned off = (idx * 4 + dt) * 512 + (lane & 7) * 64;
      unsigned sink = 0;
      if (lane < 8 && off < len) sink = *(const GAS unsigned*)(pc + off);
      if (0 && lane >= 8 && lane < 8 + 12) sink = *(const GAS unsigned*)(ws + (size_t)(lane - 8) * (2u << 20) + (1u << 20) - 4096 + idx * 64);
      asm volatile("" :: "v"(sink));
    }
    if (!tA) { bar(); pull_k(0); bar(); pull_k(1); }
    STAMP(1);
    f32x16 qacc[3] = {};
    const int p4s = ((p4 & 1) << 1) | (p4 >> 1);
    const unsigned a_off = (8 * hh + q4) * 256 + (((4 * dt + 2 * blk + (p4s >> 1)) ^ (q4 << 2)) * 16) + 8 * (p4s & 1);
    const unsigned x_off = r * 128 + 16 * KB + ((hh ^ ((r >> 1) & 7)) * 16);
    bf16x8 fa[4] = {}, fb[4][3] = {};
    auto read_q = [&](unsigned base, int ks) AI {
      fa[ks] = ldstr(base + a_off + ks * 4096, base + a_off + ks * 4096 + 1024);
#pragma unroll
      for (int j = 0; j < 3; ++j) fb[ks][j] = lds128(base + (x_off ^ (ks * 32)) + j * 4096);
    };
    auto mma_q = [&](int ks) AI {
#pragma unroll
      for (int j = 0; j < 3; ++j) qacc[j] = MFMA(fa[ks], fb[ks][j], qacc[j]);
    };
    NOUNROLL
    for (int it = 0; it < 2; ++it) {
#pragma unroll
      for (int a = 0; a < 5; ++a) {
        constexpr unsigned dummy = 0; (void)dummy;
        const unsigned base = area(a);
        bar(); SB();
        read_q(base, 0); read_q(base, 1); mma_q(2); ilv<3, 4>();
        read_q(base, 2); mma_q(3); ilv<3, 2>();
        read_q(base, 3); mma_q(0); ilv<3, 2>();
        mma_q(1); SB();
      }
    }
    mma_q(2); mma_q(3);
#pragma unroll
    for (int j = 0; j < 3; ++j)
#pragma unroll
      for (int sx = 0; sx < 2; ++sx) sts128(QIMG + (j * 8 + sx) * KB + dt * 2 * KB + l16, pack8(qacc[j], sx));
    if (tid < 16) *(LAS unsigned*)(size_t)(L_CNT + tid * 4) = 0u;
    if (tid == 0) *(LAS unsigned*)(size_t)L_TAG = epoch * 1664525u + 1013904223u;
    bar();
    STAMP(2);
  } else {
    const char* xb = x + (size_t)b * SQ * DM * 2;
    const char* wqh = wq + (size_t)h * 256;
    const unsigned vo_x = (lane >> 3) * 1280 + (((lane & 7) ^ u) * 16);
    const unsigned wq0 = u * 8192 + (((lane & 15) ^ (u << 2)) * 16) + dt * 16 * 8192, wq1 = wq0 + (32768 - 1024), wq2 = wq0 + 2 * (32768 - 1024), wq3 = wq0 + 3 * (32768 - 1024);
    const unsigned xb0 = (vo_x ^ ((dt & 1) << 6)) + dt * 3 * 10240, xb1 = (vo_x ^ (((dt + 1) & 1) << 6)) + dt * 3 * 10240 + (10240 - 1024), xb2 = xb0 + 2 * (10240 - 1024);
    const int rot = (b + 3 * i4 + g) % 10;
    auto issue_q = [&](int s0) AI {
      int s = s0 + rot; if (s >= 10) s -= 10;
      const unsigned base = ((AREA_TAB >> (4 * (s0 >= 5 ? s0 - 5 : s0))) & 15u) << 15;
      dma4<0>(wq0, wq1, wq2, wq3, wqh + (size_t)s * (64 * 8192), base + 4 * dt * KB);
      dma3(xb0, xb1, xb2, xb + s * 128, base + 16 * KB + 3 * dt * KB);
    };
    if (tA) {
      issue_q(0); issue_q(1); issue_q(2); waitvm<14>(); bar();
    } else {
      issue_kq(0); issue_kq(1); issue_kq(2);
      waitvm<16>(); bar(); issue_kq(3);
      waitvm<16>(); bar(); issue_q(0);
      waitvm<15>(); pull_k(0); issue_q(1);
      waitvm<14>(); pull_k(1);
      STAMP(1);
      waitvm<7>(); bar(); issue_q(2);
    }
    NOUNROLL
    for (int s0 = 0; s0 < 7; ++s0) { issue_q(s0 + 3); waitvm<14>(); bar(); }
    if (tA) {
      issue_kq(0); waitvm<15>(); bar();
      issue_kq(1); waitvm<16>(); bar();
      issue_kq(2); waitvm<16>(); bar();
      issue_kq(3); waitvm<16>(); bar();
    } else {
      waitvm<7>(); bar();
      issue_vh(0, 0); issue_vh(0, 1); waitvm<8>(); bar();
      issue_v(4); bar();
      issue_vh(5, 0); issue_vh(5, 1);
    }
    STAMP(2);
    bar();
  }

  f32x16 sacc[2][3] = {};
  {
    bf16x8 qf[2][6];
    auto rd_q6 = [&](int st) AI {
#pragma unroll
      for (int i = 0; i < 6; ++i) qf[st & 1][i] = lds128(QIMG + ((i % 3) * 8 + 2 * st + i / 3) * KB + l16);
    };
    auto s_tile = [&](int kt) AI {
      rd_q6(0); SB();
#pragma unroll
      for (int st = 0; st < 4; ++st) {
        if (st < 3) rd_q6(st + 1);
#pragma unroll
        for (int i = 0; i < 6; ++i) sacc[kt][i % 3] = MFMA(kf[kt][2 * st + i / 3], qf[st & 1][i], sacc[kt][i % 3]);
        ilv<6, 1>();
      }
    };
    if (tA) {
      if (r1) waitvm<8>(); pull_k(0); if (r1) issue_vh(0, 0);
      s_tile(0);
      if (r1) { issue_vh(0, 1); waitvm<8>(); } else bar(); pull_k(1); if (r1) issue_v(4);
      s_tile(1);
    } else {
      rd_q6(0); SB();
#pragma unroll
      for (int st = 0; st < 4; ++st) {
        if (st < 3) rd_q6(st + 1);
#pragma unroll
        for (int i = 0; i < 6; ++i) {
          sacc[0][i % 3] = MFMA(kf[0][2 * st + i / 3], qf[st & 1][i], sacc[0][i % 3]);
          sacc[1][i % 3] = MFMA(kf[1][2 * st + i / 3], qf[st & 1][i], sacc[1][i % 3]);
        }
        ilv<6, 1>();
      }
    }
  }
  const unsigned mxo = r * 32;
#pragma unroll
  for (int qb = 0; qb < 3; ++qb) {
    float m = sacc[0][qb][0];
#pragma unroll
    for (int kt = 0; kt < 2; ++kt)
#pragma unroll
      for (int i = 0; i < 16; ++i) m = fmaxf(m, sacc[kt][qb][i]);
    m = xhalf_max(m);
    if (hh == 0) *(LAS float*)(size_t)(L_MX + qb * 1024 + mxo + w * 4) = m;
  }
  if (r1) {
    if (tA) { issue_vh(5, 0); issue_vh(5, 1); waitvm<10>(); } else { issue_vh(9, 0); issue_vh(9, 1); waitvm<18>(); }
  }
  bar();
  STAMP(3);
  const float cs = 0.08838834764831845f * 1.4426950408889634f;
  auto max4 = [&](int qb, int half) AI {
    f32x4 m = *(const LAS f32x4*)(size_t)(L_MX + qb * 1024 + mxo + half * 16);
    return fmaxf(fmaxf(m[0], m[1]), fmaxf(m[2], m[3]));
  };
  float m0[3], lsum[3] = {0.f, 0.f, 0.f};
#pragma unroll
  for (int qb = 0; qb < 3; ++qb) m0[qb] = fmaxf(max4(qb, 0), max4(qb, r1));
  u32x4 pr[3][2];
  auto exp_tile = [&](int kt) AI {
#pragma unroll
    for (int qb = 0; qb < 3; ++qb) {
      const float nm = -m0[qb] * cs;
#pragma unroll
      for (int i = 0; i < 18; ++i) {
        if (i < 16) sacc[kt][qb][i] = __builtin_amdgcn_exp2f(fmaf(sacc[kt][qb][i], cs, nm));
        if (i >= 2) asm volatile("v_add_f32 %0, %0, %1" : "+v"(lsum[qb]) : "v"(sacc[kt][qb][i - 2]));
        if (i >= 2 && (i & 1)) pr[qb][(i - 2) >> 3][((i - 2) >> 1) & 3] = pk2(sacc[kt][qb][i - 3], sacc[kt][qb][i - 2]);
        SB();
      }
      pin(pr[qb][0]); pin(pr[qb][1]);
    }
  };
  auto put_p = [&](int kt) AI {
#pragma unroll
    for (int qb = 0; qb < 3; ++qb)
#pragma unroll
      for (int sx = 0; sx < 2; ++sx) sts128((kt == 0 ? PA : PB) + dt * 6 * KB + l16 + (sx * 3 + qb) * KB, pr[qb][sx]);
  };
  const char* wob = wo + (size_t)(4 * g) * 128 * 1280 + c4 * 320;
  const unsigned wa0 = u * 1280 + (((lane & 15) ^ (u << 2)) * 16) + dt * 32 * 1280, wa1 = wa0 + (5120 - 1024), wa2 = wa0 + 2 * (5120 - 1024), wa3 = wa0 + 3 * (5120 - 1024);
  auto issue_wa = [&](int jj) AI {
    const char* src = wob + (size_t)(((i4 + jj) & 3) * 128) * 1280;
    const unsigned d = (jj & 1) ? S(3) + dt * 8 * KB : (dt == 0 ? S(16) : (dt - 1) * 8 * KB);
    dma4<0>(wa0, wa1, wa2, wa3, src, d);
    dma4<0>(wa0, wa1, wa2, wa3, src + 16 * 1280, d + 4096);
  };
  if (r1 && tA) issue_vh(9, 0);
  exp_tile(0);
  if (r1) {
    if (tA) issue_vh(9, 1);
    cnt_wait(C_REL0); issue_v(13); issue_v(14); issue_v(15);
    waitvm<14>();
    bar();
  }
  put_p(0);
  exp_tile(1);
#pragma unroll
  for (int qb = 0; qb < 3; ++qb) {
    const float s = xhalf_sum(lsum[qb]);
    if (hh == 0) *(LAS float*)(size_t)(L_SM + qb * 1024 + mxo + w * 4) = s;
  }
  if (r1) { cnt_wait(C_REL0 + 1); issue_wa(0); waitvm<14>(); }
  STAMP(4);
  bar();
  STAMP(5);
  put_p(1);

  const unsigned v_off = (4 * hh + q4) * 256 + (((4 * dt + 2 * blk + (p4 >> 1)) ^ (q4 << 2)) * 16) + 8 * (p4 & 1);
  const unsigned tag = __builtin_amdgcn_readfirstlane(*(const LAS unsigned*)(size_t)L_TAG);
  f32x16 acc[3] = {};
  if (!r1) {
    f32x16 oa[2][3] = {};
    bf16x8 vf[4][2], pf[2][2][3];
    auto rd_v = [&](int j) AI {
#pragma unroll
      for (int sx = 0; sx < 2; ++sx) vf[j & 3][sx] = ldstr(vslot(j) + v_off + sx * 4096, vslot(j) + v_off + sx * 4096 + 2048);
    };
    auto rd_p = [&](int j) AI {
#pragma unroll
      for (int sx = 0; sx < 2; ++sx)
#pragma unroll
        for (int qb = 0; qb < 3; ++qb) pf[j & 1][sx][qb] = lds128((((j >> 2) & 1) ? PB : PA) + ((j & 3) * 6 + sx * 3 + qb) * KB + l16);
    };
    auto mma_pv = [&](int j) AI {
#pragma unroll
      for (int sx = 0; sx < 2; ++sx)
#pragma unroll
        for (int qb = 0; qb < 3; ++qb) oa[j >> 3][qb] = MFMA(vf[j & 3][sx], pf[j & 1][sx][qb], oa[j >> 3][qb]);
    };
    auto pv_quarter = [&](int q) AI {
      rd_v(4 * q); rd_p(4 * q); rd_v(4 * q + 1); rd_v(4 * q + 2);
      if (q > 0) { mma_pv(4 * q - 1); ilv<6, 3>(); } else SB();
      rd_v(4 * q + 3); rd_p(4 * q + 1); mma_pv(4 * q); ilv<6, 2>();
      rd_p(4 * q + 2); mma_pv(4 * q + 1); ilv<6, 1>();
      asm volatile("s_waitcnt lgkmcnt(6)" ::: "memory"); cnt_add(C_REL0 + q); SB();
      rd_p(4 * q + 3); mma_pv(4 * q + 2); ilv<6, 1>();
    };
    pv_quarter(0); bar(); SB();
    pv_quarter(1); bar(); SB();
    STAMP(6);
    pv_quarter(2);
    float sc0[3], sc1[3];
#pragma unroll
    for (int qb = 0; qb < 3; ++qb) {
      const float m = fmaxf(m0[qb], max4(qb, 1));
      const float al = __builtin_amdgcn_exp2f((m0[qb] - m) * cs);
      f32x4 l0 = *(const LAS f32x4*)(size_t)(L_SM + qb * 1024 + mxo);
      f32x4 l1 = *(const LAS f32x4*)(size_t)(L_SM + qb * 1024 + mxo + 16);
      const float l = al * ((l0[0] + l0[1]) + (l0[2] + l0[3])) + ((l1[0] + l1[1]) + (l1[2] + l1[3]));
      sc1[qb] = 1.0f / l; sc0[qb] = al * sc1[qb];
    }
    bar(); SB();
    STAMP(7);
    pv_quarter(3);
#pragma unroll
    for (int qb = 0; qb < 3; ++qb)
#pragma unroll
      for (int i = 0; i < 16; ++i) oa[0][qb][i] *= sc0[qb];
    mma_pv(15);
    const char* obase = ws + WS_O + (size_t)(b * NH + 4 * g) * 24576;
    const __amdgpu_buffer_rsrc_t ors = __builtin_amdgcn_make_buffer_rsrc((char*)obase, 0, 4 * 24576, 0x00020000);
    cnt_wait(C_REL0 + 3);
#pragma unroll
    for (int qb = 0; qb < 3; ++qb) {
#pragma unroll
      for (int i = 0; i < 16; ++i) oa[1][qb][i] = fmaf(oa[1][qb][i], sc1[qb], oa[0][qb][i]);
#pragma unroll
      for (int sx = 0; sx < 2; ++sx) {
        const u32x4 o = pack8(oa[1][qb], sx);
        st_sc1(ors, i4 * 24576 + (qb * 8 + sx) * KB + dt * 2 * KB + l16, o);
        sts128(oimg(0) + (qb * 8 + sx) * KB + dt * 2 * KB + l16, o);
      }
    }
    waitlds(); cnt_add(C_O0);
    STAMP(8);
    waitvm<0>();
    GAS unsigned* fl = (GAS unsigned*)(ws + WS_F1 + (size_t)(b * 8 + g) * 64);
    if (lane == 0) __hip_atomic_store(fl + dt * 4 + i4, tag, RLX_AGENT);
    STAMP(9);
    if (w == 3) {
      unsigned pend = 0xEu;
      cnt_wait(C_O0);
      NOUNROLL
      for (unsigned spin = 0; pend && spin < SPIN_CAP; ++spin) {
        const unsigned fv = __hip_atomic_load(fl + (lane & 12) + ((i4 + lane) & 3), RLX_AGENT);
        const unsigned m = (unsigned)__ballot(fv == tag);
        const unsigned rdy = m & (m >> 4) & (m >> 8) & (m >> 12) & pend;
        if (!rdy) { __builtin_amdgcn_s_sleep(1); continue; }
        NOUNROLL
        for (unsigned t = rdy; t; t &= t - 1) {
          const unsigned jj = __builtin_ctz(t);
          if (jj >= 2) cnt_wait(C_FREEX + jj - 2);
          const char* src = obase + ((i4 + jj) & 3) * 24576;
          const unsigned d = ((OIMG_TAB >> (4 * jj)) & 15u) << 13;
          NOUNROLL
          for (int i = 0; i < 6; ++i) dma4<2>(l16, l16, l16, l16, src + i * 4096, d + i * 4096);
        }
        waitvm<0>();
        NOUNROLL
        for (unsigned t = rdy; t; t &= t - 1) cnt_add(__builtin_ctz(t), 4);
        pend &= ~rdy;
      }
      if (i4 == 0 && lane == 0) __hip_atomic_store((GAS unsigned*)(ws + WS_EPOCH + (b * 8 + g) * 64), tag, RLX_AGENT);
    } else {
      const unsigned w_off = (4 * hh + q4) * 64 + (2 * blk + (p4 >> 1)) * 16 + 8 * (p4 & 1);
      cnt_wait(C_WOB);
      NOUNROLL
      for (int jj = 0; jj < 4; ++jj) {
        cnt_wait(jj);
        const unsigned wb = (jj == 0 ? S(15) : S(16) + jj * 8 * KB) + w_off;
        const unsigned ob = (((OIMG_TAB >> (4 * jj)) & 15u) << 13) + dt * 8 * KB + l16;
        bf16x8 af[8], of[8];
#pragma unroll
        for (int ds = 0; ds < 8; ++ds) { af[ds] = ldstr(wb + ds * KB, wb + ds * KB + 512); of[ds] = lds128(ob + ds * KB); }
#pragma unroll
        for (int ds = 0; ds < 8; ++ds) acc[0] = MFMA(af[ds], of[ds], acc[0]);
      }
    }
  } else {
    bf16x8 wf[4][8];
    auto pull_wa = [&](int jj) AI {
#pragma unroll
      for (int ds = 0; ds < 8; ++ds) { const unsigned a = wset(jj & 1, ds >> 1) + v_off + (ds & 1) * 4096; wf[jj][ds] = ldstr(a, a + 2048); }
    };
    auto mma_o = [&](int jj) AI {
      bf16x8 of[3][3];
#pragma unroll
      for (int ds = 0; ds < 2; ++ds)
#pragma unroll
        for (int qb = 0; qb < 3; ++qb) of[ds][qb] = lds128(oimg(jj) + (qb * 8 + ds) * KB + l16);
      SB();
#pragma unroll
      for (int ds = 0; ds < 8; ++ds) {
        if (ds < 6) {
#pragma unroll
          for (int qb = 0; qb < 3; ++qb) of[(ds + 2) % 3][qb] = lds128(oimg(jj) + (qb * 8 + ds + 2) * KB + l16);
        }
#pragma unroll
        for (int qb = 0; qb < 3; ++qb) acc[qb] = MFMA(wf[jj][ds], of[ds % 3][qb], acc[qb]);
        ilv<3, 1>();
      }
    };
    cnt_wait(C_REL0 + 2); issue_wa(1);
    waitvm<16>();
    bar();
    STAMP(6);
    {
      const unsigned wb0 = (lane >> 2) * 1280 + (lane & 3) * 16 + 256, wb1 = wb0 + (20480 - 1024), wb2 = wb0 + 2 * (20480 - 1024), wb3 = wb0 + 3 * (20480 - 1024);
      const char* src = wob + (size_t)(((i4 + dt) & 3) * 128) * 1280;
      const unsigned d = dt == 0 ? S(15) : S(16) + dt * 8 * KB;
      dma4<0>(wb0, wb1, wb2, wb3, src, d);
      dma4<0>(wb0, wb1, wb2, wb3, src + 64 * 1280, d + 4096);
    }
    waitvm<8>(); cnt_add(C_L01); cnt_wait(C_L01);
    pull_wa(0); pull_wa(1); waitlds(); cnt_add(C_P01); cnt_wait(C_P01);
    STAMP(7);
    issue_wa(2); issue_wa(3);
    waitvm<16>(); cnt_add(C_WOB);
    waitvm<8>(); cnt_add(C_L2); cnt_wait(C_L2); pull_wa(2); waitlds(); cnt_add(C_FREEX);
    waitvm<0>(); cnt_add(C_L3); cnt_wait(C_L3); pull_wa(3); waitlds(); cnt_add(C_FREEY);
    STAMP(8);
    cnt_wait(0); mma_o(0);
    STAMP(9);
    cnt_wait(1); mma_o(1);
    STAMP(10);
    cnt_wait(2); mma_o(2);
    STAMP(11);
    cnt_wait(3); mma_o(3);
  }
  STAMP(12);
  if (w != 3) {
    const __amdgpu_buffer_rsrc_t prs = __builtin_amdgcn_make_buffer_rsrc(ws + WS_P + (size_t)(b * 4 + c4) * PBYTES, 0, PBYTES, 0x00020000);
    const unsigned so = ((r >> 2) * 15 + (r1 ? 3 * dt : 12 + dt)) * 2 * KB + (g * 8 + (r & 3) + 4 * hh) * 16;
#pragma unroll
    for (int t = 0; t < 3; ++t) if (t == 0 || r1) {
#pragma unroll
      for (int uu = 0; uu < 2; ++uu) st_sc1(prs, so + (t * 2 + uu) * KB, pack8(acc[t], uu));
    }
  }
}

__global__ __launch_bounds__(256) void k_reduce(const char* __restrict__ ws, char* __restrict__ out) {
  asm volatile(".Lk2_begin:" :: "s"(ws), "s"(out));
  const unsigned t = threadIdx.x, lane = t & 63;
  const unsigned grp = blockIdx.x * 4 + (t >> 6), nt = blockIdx.y, qb = blockIdx.z;
  const char* pb = ws + WS_P + (size_t)((grp * 15 + nt * 3 + qb) * 2048 + lane * 16);
  const u32x4 t0 = *(const u32x4*)pb, t1 = *(const u32x4*)(pb + KB);
  const unsigned oo = (((grp >> 5) * SQ + 32 * qb + 4 * (grp & 7) + (lane & 3)) * DM + 160 * ((grp >> 3) & 3) + 32 * nt + 8 * (lane >> 4) + (lane & 4)) * 2;
  float y[8], z[4];
#pragma unroll
  for (int i = 0; i < 4; ++i) {
    auto q = __builtin_amdgcn_permlane32_swap(t0[i], t1[i], false, false);
    y[2 * i] = __uint_as_float(q[0] << 16) + __uint_as_float(q[1] << 16);
    y[2 * i + 1] = __uint_as_float(q[0] & 0xffff0000u) + __uint_as_float(q[1] & 0xffff0000u);
  }
#pragma unroll
  for (int i = 0; i < 4; ++i) { auto q = __builtin_amdgcn_permlane16_swap(__float_as_uint(y[i]), __float_as_uint(y[i + 4]), false, false); z[i] = __uint_as_float(q[0]) + __uint_as_float(q[1]); }
#pragma unroll
  for (int i = 0; i < 4; ++i) z[i] += __uint_as_float(__builtin_amdgcn_update_dpp(0u, __float_as_uint(z[i]), 0x128, 0xf, 0xf, false));
  const u32x2 o = {pk2(z[0], z[1]), pk2(z[2], z[3])};
  if ((lane & 8) == 0) *(u32x2*)(out + oo) = o;
  asm volatile(".Lk2_end:");
}

extern "C" void kernel_launch(void* const* d_in, const int* in_sizes, int n_in, void* d_out, int out_size, void* d_ws, size_t ws_size,
                              hipStream_t stream) {
  (void)hipFuncSetAttribute((const void*)k_fused, hipFuncAttributeMaxDynamicSharedMemorySize, LDS_BYTES);
  hipLaunchKernelGGL(k_fused, dim3(256), dim3(512), LDS_BYTES, stream, (const char*)d_in[0], (const char*)d_in[1], (const char*)d_in[2],
                     (const char*)d_in[3], (const char*)d_in[4], (char*)d_ws, (char*)d_out);
  hipLaunchKernelGGL(k_reduce, dim3(64, 5, 3), dim3(256), 0, stream, (const char*)d_ws, (char*)d_out);
}
```

```cpp
#include <hip/hip_runtime.h>
#include <hip/hip_bf16.h>

typedef __attribute__((ext_vector_type(8))) __bf16 bf16x8;
typedef __attribute__((ext_vector_type(4))) __bf16 bf16x4;
typedef __attribute__((ext_vector_type(2))) __bf16 bf16x2;
typedef __attribute__((ext_vector_type(16))) float f32x16;
typedef __attribute__((ext_vector_type(4))) float f32x4;
typedef __attribute__((ext_vector_type(2))) float f32x2;
typedef __attribute__((ext_vector_type(4))) unsigned u32x4;
typedef __attribute__((ext_vector_type(2))) unsigned u32x2;
#define LAS __attribute__((address_space(3)))
#define GAS __attribute__((address_space(1)))
#define DI __device__ __forceinline__
#define AI __attribute__((always_inline))
#define MFMA(a, b, c) __builtin_amdgcn_mfma_f32_32x32x16_bf16((a), (b), (c), 0, 0, 0)
#define SB() __builtin_amdgcn_sched_barrier(0)
#define NOUNROLL _Pragma("clang loop unroll(disable)")

constexpr int SQ = 96, DM = 640, NH = 32, SK = 512;
constexpr unsigned KB = 1024;
constexpr unsigned LDS_BYTES = 163840;

constexpr size_t WS_EPOCH = 0;
constexpr size_t WS_F1 = 4096;
constexpr size_t WS_O = 1u << 20;
constexpr size_t WS_P = 8u << 20;
constexpr unsigned PBYTES = 8 * 15 * 2 * 1024;

#define DMA_PRE "s_mov_b32 m0, %0\n\ts_nop 0\n\t"
#define DMA4(SUF) DMA_PRE "global_load_lds_dwordx4 %2, %1" SUF "\n\tglobal_load_lds_dwordx4 %3, %1 offset:1024" SUF \
                  "\n\tglobal_load_lds_dwordx4 %4, %1 offset:2048" SUF "\n\tglobal_load_lds_dwordx4 %5, %1 offset:3072" SUF
template <int POL> DI void dma4(unsigned v0, unsigned v1, unsigned v2, unsigned v3, const void* sbase, unsigned lds) {
  lds = __builtin_amdgcn_readfirstlane(lds);
  if constexpr (POL == 1) asm volatile(DMA4(" nt") :: "s"(lds), "s"(sbase), "v"(v0), "v"(v1), "v"(v2), "v"(v3) : "memory");
  else if constexpr (POL == 2) asm volatile(DMA4(" sc1") :: "s"(lds), "s"(sbase), "v"(v0), "v"(v1), "v"(v2), "v"(v3) : "memory");
  else asm volatile(DMA4("") :: "s"(lds), "s"(sbase), "v"(v0), "v"(v1), "v"(v2), "v"(v3) : "memory");
}
DI void dma3(unsigned v0, unsigned v1, unsigned v2, const void* sbase, unsigned lds) {
  lds = __builtin_amdgcn_readfirstlane(lds);
  asm volatile(DMA_PRE "global_load_lds_dwordx4 %2, %1\n\tglobal_load_lds_dwordx4 %3, %1 offset:1024\n\tglobal_load_lds_dwordx4 %4, %1 offset:2048"
               :: "s"(lds), "s"(sbase), "v"(v0), "v"(v1), "v"(v2) : "memory");
}
DI void dma2nt(unsigned v0, const void* sbase, unsigned lds) {
  lds = __builtin_amdgcn_readfirstlane(lds);
  asm volatile(DMA_PRE "global_load_lds_dwordx4 %2, %1 nt\n\tglobal_load_lds_dwordx4 %2, %1 offset:1024 nt" :: "s"(lds), "s"(sbase), "v"(v0) : "memory");
}
template <int N> DI void waitvm() { asm volatile("s_waitcnt vmcnt(%0)" ::"i"(N) : "memory"); }
DI void bar() { asm volatile("s_waitcnt lgkmcnt(0)\n\ts_barrier" ::: "memory"); }
DI void waitlds() { asm volatile("s_waitcnt lgkmcnt(0)" ::: "memory"); }
DI unsigned pk2(float a, float b) { f32x2 t = {a, b}; bf16x2 p = __builtin_convertvector(t, bf16x2); return __builtin_bit_cast(unsigned, p); }
DI bf16x8 lds128(unsigned a) { return *(const LAS bf16x8*)(size_t)a; }
DI void sts128(unsigned a, u32x4 v) { *(LAS u32x4*)(size_t)a = v; }
DI bf16x8 ldstr(unsigned a0, unsigned a1) {
  bf16x4 a = __builtin_amdgcn_ds_read_tr16_b64_v4bf16((LAS bf16x4*)(size_t)a0);
  bf16x4 b = __builtin_amdgcn_ds_read_tr16_b64_v4bf16((LAS bf16x4*)(size_t)a1);
  return __builtin_shufflevector(a, b, 0, 1, 2, 3, 4, 5, 6, 7);
}
DI float xhalf_max(float v) { auto r = __builtin_amdgcn_permlane32_swap(__float_as_uint(v), __float_as_uint(v), false, false); return fmaxf(__uint_as_float(r[0]), __uint_as_float(r[1])); }
DI float xhalf_sum(float v) { auto r = __builtin_amdgcn_permlane32_swap(__float_as_uint(v), __float_as_uint(v), false, false); return __uint_as_float(r[0]) + __uint_as_float(r[1]); }
DI u32x4 pack8(const f32x16& a, int s) {
  u32x4 o = {pk2(a[8 * s], a[8 * s + 1]), pk2(a[8 * s + 2], a[8 * s + 3]), pk2(a[8 * s + 4], a[8 * s + 5]), pk2(a[8 * s + 6], a[8 * s + 7])};
  return o;
}
DI void pin(u32x4& x) { asm volatile("" : "+v"(x)); }
template <int NM, int ND> DI void ilv() {
#pragma unroll
  for (int i = 0; i < NM; ++i) { __builtin_amdgcn_sched_group_barrier(0x008, 1, 0); __builtin_amdgcn_sched_group_barrier(0x100, ND, 0); }
  __builtin_amdgcn_sched_barrier(0);
}
DI void st_sc1(__amdgpu_buffer_rsrc_t rs, unsigned off, u32x4 v) { __builtin_amdgcn_raw_buffer_store_b128(v, rs, off, 0, 16); }
#define RLX_AGENT __ATOMIC_RELAXED, __HIP_MEMORY_SCOPE_AGENT
constexpr unsigned SPIN_CAP = 1u << 17;

constexpr unsigned S(int i) { return 8 * KB * i; }
constexpr unsigned area(int a) { return a == 0 ? S(12) : a == 1 ? S(16) : a == 2 ? S(8) : a == 3 ? S(0) : S(4); }
constexpr unsigned AREA_TAB = 0x10243u;
constexpr unsigned QIMG = S(8), PB = S(8), PA = S(17), MISC = S(11);
constexpr unsigned L_MX = MISC, L_SM = MISC + 3072, L_TAG = MISC + 6144, L_CNT = MISC + 6400;
constexpr unsigned vslot(int j) { return j < 5 ? S(12 + j) : j < 13 ? S(j - 5) : S(12 + j - 13); }
constexpr unsigned oimg(int jj) { return jj == 0 ? S(12) : jj == 1 ? S(8) : jj == 2 ? S(0) : S(4); }
constexpr unsigned OIMG_TAB = 0x408Cu;
constexpr unsigned wset(int set, int t) { return set == 0 ? (t == 0 ? S(16) : S(t - 1)) : S(3 + t); }
enum { C_O0 = 0, C_FREEX = 4, C_FREEY = 5, C_WOB = 6, C_REL0 = 7, C_L01 = 11, C_P01 = 12, C_L2 = 13, C_L3 = 14 };
DI void cnt_add(unsigned idx, unsigned n = 1) {
  unsigned long long keep; const unsigned a = L_CNT + idx * 4;
  asm volatile("s_mov_b64 %0, exec\n\ts_mov_b64 exec, 1\n\tds_add_u32 %1, %2\n\ts_mov_b64 exec, %0" : "=&s"(keep) : "v"(a), "v"(n) : "memory");
}
DI void cnt_wait(unsigned idx) {
  const unsigned a = L_CNT + idx * 4; unsigned v, s, n = 1u << 22;
  asm volatile("1:\n\tds_read_b32 %0, %3\n\ts_waitcnt lgkmcnt(0)\n\tv_readfirstlane_b32 %1, %0\n\ts_cmp_ge_u32 %1, 4\n\ts_cbranch_scc1 2f\n\t"
               "s_sleep 1\n\ts_sub_u32 %2, %2, 1\n\ts_cmp_lg_u32 %2, 0\n\ts_cbranch_scc1 1b\n2:"
               : "=&v"(v), "=&s"(s), "+s"(n) : "v"(a) : "memory", "scc");
}

__global__ __launch_bounds__(512, 2) void k_fused(const char* __restrict__ x, const char* __restrict__ kc, const char* __restrict__ vc,
                                                   const char* __restrict__ wq, const char* __restrict__ wo, char* __restrict__ ws, char* __restrict__ out) {
  const int g = blockIdx.x & 7, idx = blockIdx.x >> 3, b = idx >> 2, i4 = idx & 3, h = 4 * g + i4;
  const int c4 = (i4 + (b >> 1)) & 3;
  const bool tA = (i4 & 1) == 0;
  const int tid = threadIdx.x, lane = tid & 63;
  const int w = __builtin_amdgcn_readfirstlane(tid >> 6), dt = w & 3;
  const bool r1 = w >= 4;
  const int r = lane & 31, hh = lane >> 5;
  const int blk = (lane >> 4) & 1, q4 = (lane & 15) >> 2, p4 = lane & 3;
  const unsigned u = lane >> 4, l16 = lane * 16;
  const char* kb = kc + ((size_t)(b * NH + h) * SK) * 256;
  const char* vb = vc + ((size_t)(b * NH + h) * SK) * 256;
  const unsigned vo_tr = u * 256 + (((lane & 15) ^ (u << 2)) * 16);
  const unsigned vqv = vo_tr + dt * 8192, vtv = vo_tr + dt * 2048;
  const unsigned k0v = u * 256 + (((lane & 15) ^ u) * 16) + dt * 8192, k1v = k0v ^ 64, k2v = k0v ^ 128, k3v = k0v ^ 192;
  auto karea = [&](int q) AI { return (q < 2 && tA) ? area(q) : area(q + 1); };
  auto issue_kq = [&](int q) AI {
    const unsigned d = karea(q) + dt * 8 * KB;
    dma4<1>(k0v, k1v, k2v, k3v, kb + q * 32768, d);
    dma4<1>(k0v, k1v, k2v, k3v, kb + q * 32768 + 4096, d + 4096);
  };
  auto issue_v = [&](int j) AI { dma2nt(vtv, vb + j * 8192, vslot(j) + dt * 2 * KB); };
  auto issue_vh = [&](int j0, int half) AI { dma4<1>(vqv, vqv, vqv, vqv, vb + j0 * 8192 + half * 4096, vslot(j0) + dt * 8 * KB + half * 4096); };
  bf16x8 kf[2][8];
  const unsigned kfo = dt * 8 * KB + r * 256 + ((hh ^ (r & 15)) * 16);
  auto pull_k = [&](int kt) AI {
    const unsigned slot = (r1 ? area(3 + kt) : karea(kt)) + kfo;
#pragma unroll
    for (int s = 0; s < 8; ++s) kf[kt][s] = lds128(slot ^ (s * 32));
  };

  if (!r1) {
    const unsigned epoch = __hip_atomic_load((GAS unsigned*)(ws + WS_EPOCH + (b * 8 + g) * 64), RLX_AGENT);
    {
      unsigned long long pc; unsigned len;
      asm volatile("s_getpc_b64 %0\n.Lk1_here:\n\ts_mov_b32 %1, .Lk2_end-.Lk1_here" : "=s"(pc), "=s"(len));
      const unsigned off = (idx * 4 + dt) * 512 + (lane & 7) * 64;
      unsigned sink = 0;
      if (lane < 8 && off < len) sink = *(const GAS unsigned*)(pc + off);
      if (0 && lane >= 8 && lane < 8 + 12) sink = *(const GAS unsigned*)(ws + (size_t)(lane - 8) * (2u << 20) + (1u << 20) - 4096 + idx * 64);
      asm volatile("" :: "v"(sink));
    }
    if (!tA) { bar(); pull_k(0); bar(); pull_k(1); }
    f32x16 qacc[3] = {};
    const int p4s = ((p4 & 1) << 1) | (p4 >> 1);
    const unsigned a_off = (8 * hh + q4) * 256 + (((4 * dt + 2 * blk + (p4s >> 1)) ^ (q4 << 2)) * 16) + 8 * (p4s & 1);
    const unsigned x_off = r * 128 + 16 * KB + ((hh ^ ((r >> 1) & 7)) * 16);
    bf16x8 fa[4] = {}, fb[4][3] = {};
    auto read_q = [&](unsigned base, int ks) AI {
      fa[ks] = ldstr(base + a_off + ks * 4096, base + a_off + ks * 4096 + 1024);
#pragma unroll
      for (int j = 0; j < 3; ++j) fb[ks][j] = lds128(base + (x_off ^ (ks * 32)) + j * 4096);
    };
    auto mma_q = [&](int ks) AI {
#pragma unroll
      for (int j = 0; j < 3; ++j) qacc[j] = MFMA(fa[ks], fb[ks][j], qacc[j]);
    };
    NOUNROLL
    for (int it = 0; it < 2; ++it) {
#pragma unroll
      for (int a = 0; a < 5; ++a) {
        const unsigned base = area(a);
        bar(); SB();
        read_q(base, 0); read_q(base, 1); mma_q(2); ilv<3, 4>();
        read_q(base, 2); mma_q(3); ilv<3, 2>();
        read_q(base, 3); mma_q(0); ilv<3, 2>();
        mma_q(1); SB();
      }
    }
    mma_q(2); mma_q(3);
#pragma unroll
    for (int j = 0; j < 3; ++j)
#pragma unroll
      for (int sx = 0; sx < 2; ++sx) sts128(QIMG + (j * 8 + sx) * KB + dt * 2 * KB + l16, pack8(qacc[j], sx));
    if (tid < 16) *(LAS unsigned*)(size_t)(L_CNT + tid * 4) = 0u;
    if (tid == 0) *(LAS unsigned*)(size_t)L_TAG = epoch * 1664525u + 1013904223u;
    bar();
  } else {
    const char* xb = x + (size_t)b * SQ * DM * 2;
    const char* wqh = wq + (size_t)h * 256;
    const unsigned vo_x = (lane >> 3) * 1280 + (((lane & 7) ^ u) * 16);
    const unsigned wq0 = u * 8192 + (((lane & 15) ^ (u << 2)) * 16) + dt * 16 * 8192, wq1 = wq0 + (32768 - 1024), wq2 = wq0 + 2 * (32768 - 1024), wq3 = wq0 + 3 * (32768 - 1024);
    const unsigned xb0 = (vo_x ^ ((dt & 1) << 6)) + dt * 3 * 10240, xb1 = (vo_x ^ (((dt + 1) & 1) << 6)) + dt * 3 * 10240 + (10240 - 1024), xb2 = xb0 + 2 * (10240 - 1024);
    const int rot = (b + 3 * i4 + g) % 10;
    auto issue_q = [&](int s0) AI {
      int s = s0 + rot; if (s >= 10) s -= 10;
      const unsigned base = ((AREA_TAB >> (4 * (s0 >= 5 ? s0 - 5 : s0))) & 15u) << 15;
      dma4<0>(wq0, wq1, wq2, wq3, wqh + (size_t)s * (64 * 8192), base + 4 * dt * KB);
      dma3(xb0, xb1, xb2, xb + s * 128, base + 16 * KB + 3 * dt * KB);
    };
    if (tA) {
      issue_q(0); issue_q(1); issue_q(2); waitvm<14>(); bar();
    } else {
      issue_kq(0); issue_kq(1); issue_kq(2);
      waitvm<16>(); bar(); issue_kq(3);
      waitvm<16>(); bar(); issue_q(0);
      waitvm<15>(); pull_k(0); issue_q(1);
      waitvm<14>(); pull_k(1);
      waitvm<7>(); bar(); issue_q(2);
    }
    NOUNROLL
    for (int s0 = 0; s0 < 7; ++s0) { issue_q(s0 + 3); waitvm<14>(); bar(); }
    if (tA) {
      issue_kq(0); waitvm<15>(); bar();
      issue_kq(1); waitvm<16>(); bar();
      issue_kq(2); waitvm<16>(); bar();
      issue_kq(3); waitvm<16>(); bar();
    } else {
      waitvm<7>(); bar();
      issue_vh(0, 0); issue_vh(0, 1); waitvm<8>(); bar();
      issue_v(4); bar();
      issue_vh(5, 0); issue_vh(5, 1);
    }
    bar();
  }

  f32x16 sacc[2][3] = {};
  {
    bf16x8 qf[2][6];
    auto rd_q6 = [&](int st) AI {
#pragma unroll
      for (int i = 0; i < 6; ++i) qf[st & 1][i] = lds128(QIMG + ((i % 3) * 8 + 2 * st + i / 3) * KB + l16);
    };
    auto s_tile = [&](int kt) AI {
      rd_q6(0); SB();
#pragma unroll
      for (int st = 0; st < 4; ++st) {
        if (st < 3) rd_q6(st + 1);
#pragma unroll
        for (int i = 0; i < 6; ++i) sacc[kt][i % 3] = MFMA(kf[kt][2 * st + i / 3], qf[st & 1][i], sacc[kt][i % 3]);
        ilv<6, 1>();
      }
    };
    if (tA) {
      if (r1) waitvm<8>(); pull_k(0); if (r1) issue_vh(0, 0);
      s_tile(0);
      if (r1) { issue_vh(0, 1); waitvm<8>(); } else bar(); pull_k(1); if (r1) issue_v(4);
      s_tile(1);
    } else {
      rd_q6(0); SB();
#pragma unroll
      for (int st = 0; st < 4; ++st) {
        if (st < 3) rd_q6(st + 1);
#pragma unroll
        for (int i = 0; i < 6; ++i) {
          sacc[0][i % 3] = MFMA(kf[0][2 * st + i / 3], qf[st & 1][i], sacc[0][i % 3]);
          sacc[1][i % 3] = MFMA(kf[1][2 * st + i / 3], qf[st & 1][i], sacc[1][i % 3]);
        }
        ilv<6, 1>();
      }
    }
  }
  const unsigned mxo = r * 32;
#pragma unroll
  for (int qb = 0; qb < 3; ++qb) {
    float m = sacc[0][qb][0];
#pragma unroll
    for (int kt = 0; kt < 2; ++kt)
#pragma unroll
      for (int i = 0; i < 16; ++i) m = fmaxf(m, sacc[kt][qb][i]);
    m = xhalf_max(m);
    if (hh == 0) *(LAS float*)(size_t)(L_MX + qb * 1024 + mxo + w * 4) = m;
  }
  if (r1) {
    if (tA) { issue_vh(5, 0); issue_vh(5, 1); waitvm<10>(); } else { issue_vh(9, 0); issue_vh(9, 1); waitvm<18>(); }
  }
  bar();
  const float cs = 0.08838834764831845f * 1.4426950408889634f;
  auto max4 = [&](int qb, int half) AI {
    f32x4 m = *(const LAS f32x4*)(size_t)(L_MX + qb * 1024 + mxo + half * 16);
    return fmaxf(fmaxf(m[0], m[1]), fmaxf(m[2], m[3]));
  };
  float m0[3], lsum[3] = {0.f, 0.f, 0.f};
#pragma unroll
  for (int qb = 0; qb < 3; ++qb) m0[qb] = fmaxf(max4(qb, 0), max4(qb, r1));
  u32x4 pr[3][2];
  auto exp_tile = [&](int kt) AI {
#pragma unroll
    for (int qb = 0; qb < 3; ++qb) {
      const float nm = -m0[qb] * cs;
#pragma unroll
      for (int i = 0; i < 18; ++i) {
        if (i < 16) sacc[kt][qb][i] = __builtin_amdgcn_exp2f(fmaf(sacc[kt][qb][i], cs, nm));
        if (i >= 2) asm volatile("v_add_f32 %0, %0, %1" : "+v"(lsum[qb]) : "v"(sacc[kt][qb][i - 2]));
        if (i >= 2 && (i & 1)) pr[qb][(i - 2) >> 3][((i - 2) >> 1) & 3] = pk2(sacc[kt][qb][i - 3], sacc[kt][qb][i - 2]);
        SB();
      }
      pin(pr[qb][0]); pin(pr[qb][1]);
    }
  };
  auto put_p = [&](int kt) AI {
#pragma unroll
    for (int qb = 0; qb < 3; ++qb)
#pragma unroll
      for (int sx = 0; sx < 2; ++sx) sts128((kt == 0 ? PA : PB) + dt * 6 * KB + l16 + (sx * 3 + qb) * KB, pr[qb][sx]);
  };
  const char* wob = wo + (size_t)(4 * g) * 128 * 1280 + c4 * 320;
  const unsigned wa0 = u * 1280 + (((lane & 15) ^ (u << 2)) * 16) + dt * 32 * 1280, wa1 = wa0 + (5120 - 1024), wa2 = wa0 + 2 * (5120 - 1024), wa3 = wa0 + 3 * (5120 - 1024);
  auto issue_wa = [&](int jj) AI {
    const char* src = wob + (size_t)(((i4 + jj) & 3) * 128) * 1280;
    const unsigned d = (jj & 1) ? S(3) + dt * 8 * KB : (dt == 0 ? S(16) : (dt - 1) * 8 * KB);
    dma4<0>(wa0, wa1, wa2, wa3, src, d);
    dma4<0>(wa0, wa1, wa2, wa3, src + 16 * 1280, d + 4096);
  };
  if (r1 && tA) issue_vh(9, 0);
  exp_tile(0);
  if (r1) {
    if (tA) issue_vh(9, 1);
    cnt_wait(C_REL0); issue_v(13); issue_v(14); issue_v(15);
    waitvm<14>();
    bar();
  }
  put_p(0);
  exp_tile(1);
#pragma unroll
  for (int qb = 0; qb < 3; ++qb) {
    const float s = xhalf_sum(lsum[qb]);
    if (hh == 0) *(LAS float*)(size_t)(L_SM + qb * 1024 + mxo + w * 4) = s;
  }
  if (r1) { cnt_wait(C_REL0 + 1); issue_wa(0); waitvm<14>(); }
  bar();
  put_p(1);

  const unsigned v_off = (4 * hh + q4) * 256 + (((4 * dt + 2 * blk + (p4 >> 1)) ^ (q4 << 2)) * 16) + 8 * (p4 & 1);
  const unsigned tag = __builtin_amdgcn_readfirstlane(*(const LAS unsigned*)(size_t)L_TAG);
  f32x16 acc[3] = {};
  if (!r1) {
    f32x16 oa[2][3] = {};
    bf16x8 vf[4][2], pf[2][2][3];
    auto rd_v = [&](int j) AI {
#pragma unroll
      for (int sx = 0; sx < 2; ++sx) vf[j & 3][sx] = ldstr(vslot(j) + v_off + sx * 4096, vslot(j) + v_off + sx * 4096 + 2048);
    };
    auto rd_p = [&](int j) AI {
#pragma unroll
      for (int sx = 0; sx < 2; ++sx)
#pragma unroll
        for (int qb = 0; qb < 3; ++qb) pf[j & 1][sx][qb] = lds128((((j >> 2) & 1) ? PB : PA) + ((j & 3) * 6 + sx * 3 + qb) * KB + l16);
    };
    auto mma_pv = [&](int j) AI {
#pragma unroll
      for (int sx = 0; sx < 2; ++sx)
#pragma unroll
        for (int qb = 0; qb < 3; ++qb) oa[j >> 3][qb] = MFMA(vf[j & 3][sx], pf[j & 1][sx][qb], oa[j >> 3][qb]);
    };
    auto pv_quarter = [&](int q) AI {
      rd_v(4 * q); rd_p(4 * q); rd_v(4 * q + 1); rd_v(4 * q + 2);
      if (q > 0) { mma_pv(4 * q - 1); ilv<6, 3>(); } else SB();
      rd_v(4 * q + 3); rd_p(4 * q + 1); mma_pv(4 * q); ilv<6, 2>();
      rd_p(4 * q + 2); mma_pv(4 * q + 1); ilv<6, 1>();
      asm volatile("s_waitcnt lgkmcnt(6)" ::: "memory"); cnt_add(C_REL0 + q); SB();
      rd_p(4 * q + 3); mma_pv(4 * q + 2); ilv<6, 1>();
    };
    pv_quarter(0); bar(); SB();
    pv_quarter(1); bar(); SB();
    pv_quarter(2);
    float sc0[3], sc1[3];
#pragma unroll
    for (int qb = 0; qb < 3; ++qb) {
      const float m = fmaxf(m0[qb], max4(qb, 1));
      const float al = __builtin_amdgcn_exp2f((m0[qb] - m) * cs);
      f32x4 l0 = *(const LAS f32x4*)(size_t)(L_SM + qb * 1024 + mxo);
      f32x4 l1 = *(const LAS f32x4*)(size_t)(L_SM + qb * 1024 + mxo + 16);
      const float l = al * ((l0[0] + l0[1]) + (l0[2] + l0[3])) + ((l1[0] + l1[1]) + (l1[2] + l1[3]));
      sc1[qb] = 1.0f / l; sc0[qb] = al * sc1[qb];
    }
    bar(); SB();
    pv_quarter(3);
#pragma unroll
    for (int qb = 0; qb < 3; ++qb)
#pragma unroll
      for (int i = 0; i < 16; ++i) oa[0][qb][i] *= sc0[qb];
    mma_pv(15);
    const char* obase = ws + WS_O + (size_t)(b * NH + 4 * g) * 24576;
    const __amdgpu_buffer_rsrc_t ors = __builtin_amdgcn_make_buffer_rsrc((char*)obase, 0, 4 * 24576, 0x00020000);
    cnt_wait(C_REL0 + 3);
#pragma unroll
    for (int qb = 0; qb < 3; ++qb) {
#pragma unroll
      for (int i = 0; i < 16; ++i) oa[1][qb][i] = fmaf(oa[1][qb][i], sc1[qb], oa[0][qb][i]);
#pragma unroll
      for (int sx = 0; sx < 2; ++sx) {
        const u32x4 o = pack8(oa[1][qb], sx);
        st_sc1(ors, i4 * 24576 + (qb * 8 + sx) * KB + dt * 2 * KB + l16, o);
        sts128(oimg(0) + (qb * 8 + sx) * KB + dt * 2 * KB + l16, o);
      }
    }
    waitlds(); cnt_add(C_O0);
    waitvm<0>();
    GAS unsigned* fl = (GAS unsigned*)(ws + WS_F1 + (size_t)(b * 8 + g) * 64);
    if (lane == 0) __hip_atomic_store(fl + dt * 4 + i4, tag, RLX_AGENT);
    if (w == 3) {
      unsigned pend = 0xEu;
      cnt_wait(C_O0);
      NOUNROLL
      for (unsigned spin = 0; pend && spin < SPIN_CAP; ++spin) {
        const unsigned fv = __hip_atomic_load(fl + (lane & 12) + ((i4 + lane) & 3), RLX_AGENT);
        const unsigned m = (unsigned)__ballot(fv == tag);
        const unsigned rdy = m & (m >> 4) & (m >> 8) & (m >> 12) & pend;
        if (!rdy) { __builtin_amdgcn_s_sleep(1); continue; }
        NOUNROLL
        for (unsigned t = rdy; t; t &= t - 1) {
          const unsigned jj = __builtin_ctz(t);
          if (jj >= 2) cnt_wait(C_FREEX + jj - 2);
          const char* src = obase + ((i4 + jj) & 3) * 24576;
          const unsigned d = ((OIMG_TAB >> (4 * jj)) & 15u) << 13;
          NOUNROLL
          for (int i = 0; i < 6; ++i) dma4<2>(l16, l16, l16, l16, src + i * 4096, d + i * 4096);
        }
        waitvm<0>();
        NOUNROLL
        for (unsigned t = rdy; t; t &= t - 1) cnt_add(__builtin_ctz(t), 4);
        pend &= ~rdy;
      }
      if (i4 == 0 && lane == 0) __hip_atomic_store((GAS unsigned*)(ws + WS_EPOCH + (b * 8 + g) * 64), tag, RLX_AGENT);
    } else {
      const unsigned w_off = (4 * hh + q4) * 64 + (2 * blk + (p4 >> 1)) * 16 + 8 * (p4 & 1);
      cnt_wait(C_WOB);
      NOUNROLL
      for (int jj = 0; jj < 4; ++jj) {
        cnt_wait(jj);
        const unsigned wb = (jj == 0 ? S(15) : S(16) + jj * 8 * KB) + w_off;
        const unsigned ob = (((OIMG_TAB >> (4 * jj)) & 15u) << 13) + dt * 8 * KB + l16;
        bf16x8 af[8], of[8];
#pragma unroll
        for (int ds = 0; ds < 8; ++ds) { af[ds] = ldstr(wb + ds * KB, wb + ds * KB + 512); of[ds] = lds128(ob + ds * KB); }
#pragma unroll
        for (int ds = 0; ds < 8; ++ds) acc[0] = MFMA(af[ds], of[ds], acc[0]);
      }
    }
  } else {
    bf16x8 wf[4][8];
    auto pull_wa = [&](int jj) AI {
#pragma unroll
      for (int ds = 0; ds < 8; ++ds) { const unsigned a = wset(jj & 1, ds >> 1) + v_off + (ds & 1) * 4096; wf[jj][ds] = ldstr(a, a + 2048); }
    };
    auto mma_o = [&](int jj) AI {
      bf16x8 of[3][3];
#pragma unroll
      for (int ds = 0; ds < 2; ++ds)
#pragma unroll
        for (int qb = 0; qb < 3; ++qb) of[ds][qb] = lds128(oimg(jj) + (qb * 8 + ds) * KB + l16);
      SB();
#pragma unroll
      for (int ds = 0; ds < 8; ++ds) {
        if (ds < 6) {
#pragma unroll
          for (int qb = 0; qb < 3; ++qb) of[(ds + 2) % 3][qb] = lds128(oimg(jj) + (qb * 8 + ds + 2) * KB + l16);
        }
#pragma unroll
        for (int qb = 0; qb < 3; ++qb) acc[qb] = MFMA(wf[jj][ds], of[ds % 3][qb], acc[qb]);
        ilv<3, 1>();
      }
    };
    cnt_wait(C_REL0 + 2); issue_wa(1);
    waitvm<16>();
    bar();
    {
      const unsigned wb0 = (lane >> 2) * 1280 + (lane & 3) * 16 + 256, wb1 = wb0 + (20480 - 1024), wb2 = wb0 + 2 * (20480 - 1024), wb3 = wb0 + 3 * (20480 - 1024);
      const char* src = wob + (size_t)(((i4 + dt) & 3) * 128) * 1280;
      const unsigned d = dt == 0 ? S(15) : S(16) + dt * 8 * KB;
      dma4<0>(wb0, wb1, wb2, wb3, src, d);
      dma4<0>(wb0, wb1, wb2, wb3, src + 64 * 1280, d + 4096);
    }
    waitvm<8>(); cnt_add(C_L01); cnt_wait(C_L01);
    pull_wa(0); pull_wa(1); waitlds(); cnt_add(C_P01); cnt_wait(C_P01);
    issue_wa(2); issue_wa(3);
    waitvm<16>(); cnt_add(C_WOB);
    waitvm<8>(); cnt_add(C_L2); cnt_wait(C_L2); pull_wa(2); waitlds(); cnt_add(C_FREEX);
    waitvm<0>(); cnt_add(C_L3); cnt_wait(C_L3); pull_wa(3); waitlds(); cnt_add(C_FREEY);
    cnt_wait(0); mma_o(0);
    cnt_wait(1); mma_o(1);
    cnt_wait(2); mma_o(2);
    cnt_wait(3); mma_o(3);
  }
  if (w != 3) {
    const __amdgpu_buffer_rsrc_t prs = __builtin_amdgcn_make_buffer_rsrc(ws + WS_P + (size_t)(b * 4 + c4) * PBYTES, 0, PBYTES, 0x00020000);
    const unsigned so = ((r >> 2) * 15 + (r1 ? 3 * dt : 12 + dt)) * 2 * KB + (g * 8 + (r & 3) + 4 * hh) * 16;
#pragma unroll
    for (int t = 0; t < 3; ++t) if (t == 0 || r1) {
#pragma unroll
      for (int uu = 0; uu < 2; ++uu) st_sc1(prs, so + (t * 2 + uu) * KB, pack8(acc[t], uu));
    }
  }
}

__global__ __launch_bounds__(256) void k_reduce(const char* __restrict__ ws, char* __restrict__ out) {
  asm volatile("" :: "s"(ws), "s"(out));
  const unsigned t = threadIdx.x, lane = t & 63;
  const unsigned grp = blockIdx.x * 4 + (t >> 6), nt = blockIdx.y, qb = blockIdx.z;
  const char* pb = ws + WS_P + (size_t)((grp * 15 + nt * 3 + qb) * 2048 + lane * 16);
  const u32x4 t0 = *(const u32x4*)pb, t1 = *(const u32x4*)(pb + KB);
  const unsigned oo = (((grp >> 5) * SQ + 32 * qb + 4 * (grp & 7) + (lane & 3)) * DM + 160 * ((grp >> 3) & 3) + 32 * nt + 8 * (lane >> 4) + (lane & 4)) * 2;
  float y[8], z[4];
#pragma unroll
  for (int i = 0; i < 4; ++i) {
    auto q = __builtin_amdgcn_permlane32_swap(t0[i], t1[i], false, false);
    y[2 * i] = __uint_as_float(q[0] << 16) + __uint_as_float(q[1] << 16);
    y[2 * i + 1] = __uint_as_float(q[0] & 0xffff0000u) + __uint_as_float(q[1] & 0xffff0000u);
  }
#pragma unroll
  for (int i = 0; i < 4; ++i) { auto q = __builtin_amdgcn_permlane16_swap(__float_as_uint(y[i]), __float_as_uint(y[i + 4]), false, false); z[i] = __uint_as_float(q[0]) + __uint_as_float(q[1]); }
#pragma unroll
  for (int i = 0; i < 4; ++i) z[i] += __uint_as_float(__builtin_amdgcn_update_dpp(0u, __float_as_uint(z[i]), 0x128, 0xf, 0xf, false));
  const u32x2 o = {pk2(z[0], z[1]), pk2(z[2], z[3])};
  if ((lane & 8) == 0) *(u32x2*)(out + oo) = o;
  asm volatile(".Lk2_end:");
}

extern "C" void kernel_launch(void* const* d_in, const int* in_sizes, int n_in, void* d_out, int out_size, void* d_ws, size_t ws_size,
                              hipStream_t stream) {
  (void)hipFuncSetAttribute((const void*)k_fused, hipFuncAttributeMaxDynamicSharedMemorySize, LDS_BYTES);
  hipLaunchKernelGGL(k_fused, dim3(256), dim3(512), LDS_BYTES, stream, (const char*)d_in[0], (const char*)d_in[1], (const char*)d_in[2],
                     (const char*)d_in[3], (const char*)d_in[4], (char*)d_ws, (char*)d_out);
  hipLaunchKernelGGL(k_reduce, dim3(64, 5, 3), dim3(256), 0, stream, (const char*)d_ws, (char*)d_out);
}
```
